```python
import torch

def _softmax(x, axis=-1):
    return torch.softmax(x, dim=axis)

def _gelu(x):
    return torch.nn.functional.gelu(x, approximate='tanh')

def _silu(x):
    return torch.nn.functional.silu(x)

def _sigmoid(x):
    return torch.sigmoid(x)

def _layernorm(x, axis=-1, eps=1e-05):
    m = x.mean(dim=axis, keepdim=True)
    v = x.var(dim=axis, keepdim=True, unbiased=False)
    return (x - m) / torch.sqrt(v + eps)

def _scatter_add(tgt, idx, src, axis=0):
    return tgt.clone().index_add_(axis, idx, src)

def _segment_sum(src, seg, n):
    out = torch.zeros((n,) + src.shape[1:], dtype=src.dtype)
    return out.index_add_(0, seg, src)

def _pad(x, pw):
    flat = tuple((v for pair in reversed(pw) for v in pair))
    return torch.nn.functional.pad(x, flat)
kernel_name = 'adaLN_cond__ssm_conv_h3__moe_arch_mod__prompt'

def setup_inputs():

    _w = lambda a: (lambda t: t.to(torch.bfloat16) if t.is_floating_point() else t)(torch.as_tensor(a))

    def _A_setup():
        B, S, D = (1, 2048, 640)
        ks = 4
        return {'q': _w((lambda *s: torch.randn(*s, dtype=torch.bfloat16) * 0.05)(B, S, D)), 'k': _w((lambda *s: torch.randn(*s, dtype=torch.bfloat16) * 0.4)(B, S, D)), 'v': _w((lambda *s: torch.randn(*s, dtype=torch.bfloat16) * 0.4)(B, S, D)), 'shift_k': _w((lambda *s: torch.randn(*s, dtype=torch.bfloat16) * 0.4)(D, ks) * 0.5), 'ssm_k': _w((lambda *s: torch.randn(*s, dtype=torch.bfloat16) * 0.8)(D, S) * (1.0 / S ** 0.5)), 'ssm_D': _w((lambda *s: torch.randn(*s, dtype=torch.bfloat16) * 0.4)(D))}
    _A_in = _A_setup()

    def _B_setup():
        B, S, D, F, C = (1, 2048, 640, 1728, 2)
        return {'x': _w((lambda *s: torch.randn(*s, dtype=torch.bfloat16) * 0.05)(B, S, D)), 'w_r': _w((lambda *s: torch.randn(*s, dtype=torch.bfloat16) * 0.05)(D)), 'w_gate': _w((lambda *s: torch.randn(*s, dtype=torch.bfloat16) * 0.05)(D, F)), 'w_up': _w((lambda *s: torch.randn(*s, dtype=torch.bfloat16) * 0.05)(D, F)), 'w_down': _w((lambda *s: torch.randn(*s, dtype=torch.bfloat16) * 0.05)(F, D))}
    _B_in = _B_setup()
    return {'A_q': _A_in['q'], 'A_k': _A_in['k'], 'A_v': _A_in['v'], 'A_shift_k': _A_in['shift_k'], 'A_ssm_k': _A_in['ssm_k'], 'A_ssm_D': _A_in['ssm_D'], 'B_x': _B_in['x'], 'B_w_r': _B_in['w_r'], 'B_w_gate': _B_in['w_gate'], 'B_w_up': _B_in['w_up'], 'B_w_down': _B_in['w_down'], 'c': _w((lambda *s: torch.randn(*s, dtype=torch.bfloat16) * 0.2)(1, 640) * 8.0), 'w_mod': _w((lambda *s: torch.randn(*s, dtype=torch.bfloat16) * 0.2)(640, 6 * 640) * 640 ** (-0.5) * 8.0)}

def reference(A_q, A_k, A_v, A_shift_k, A_ssm_k, A_ssm_D, B_x, B_w_r, B_w_gate, B_w_up, B_w_down, c, w_mod):

    def _A_ref(q, k, v, shift_k, ssm_k, ssm_D):
        B, S, D = q.shape
        ks = shift_k.shape[-1]
        k_pad = _pad(k, ((0, 0), (ks - 1, 0), (0, 0)))
        k_shift = torch.zeros_like(k)
        for t in range(ks):
            k_shift = k_shift + k_pad[:, t:t + S, :] * shift_k[:, ks - 1 - t][None, None, :]
        kv = k_shift * v
        L = 2 * S
        kv_pad = _pad(kv, ((0, 0), (0, S), (0, 0)))
        kf_pad = _pad(ssm_k, ((0, 0), (0, S)))
        kv_f = (lambda x, axes=(-2, -1): torch.fft.rfft2(x.float(), dim=axes))(kv_pad[:, None, :, :], axes=(1, 2))
        kf_f = (lambda x, axes=(-2, -1): torch.fft.rfft2(x.float(), dim=axes))(kf_pad[:, None, :], axes=(1, 2))
        kf_f = (lambda x, *d: x.permute(*d))(kf_f, 1, 2, 0)
        y_f = kv_f * kf_f[None, :, :, :]
        y = (lambda x, s, axes=(-2, -1): torch.fft.irfft2(x, s=s, dim=axes).to(torch.bfloat16))(y_f, (1, L), axes=(1, 2))[:, 0, :S, :]
        y = y + kv * ssm_D[None, None, :]
        return q * y

    def _B_ref(x, w_r, w_gate, w_up, w_down):
        B, S, D = x.shape
        C = 2
        r = x @ w_r
        idx = torch.argsort(-r, axis=-1)[:, :C]
        idx_e = (lambda x, n, axis: torch.repeat_interleave(x, n, dim=axis))(idx.reshape(B, C, 1), D, axis=2)
        x_sel = (lambda x, i, axis: torch.take_along_dim(x, i, dim=axis))(x, idx_e, axis=1)
        g = _silu(x_sel @ w_gate)
        u = x_sel @ w_up
        y = g * u @ w_down
        r_sel = _sigmoid((lambda x, i, axis: torch.take_along_dim(x, i, dim=axis))(r, idx, axis=1))
        y = y * r_sel.reshape(B, C, 1)
        b_off = (torch.arange(B) * S).reshape(B, 1)
        flat_idx = (idx + b_off).reshape(B * C)
        zero = (x * 0.0).reshape(B * S, D)
        upd = _scatter_add(zero, flat_idx, y.reshape(B * C, D), axis=0)
        return x + upd.reshape(B, S, D)
    x = A_q
    mod = (_silu(c) @ w_mod).reshape(c.shape[0], 6, 1, -1)
    sa, ca, ga, sb, cb, gb = (mod[:, 0].clone(), mod[:, 1].clone(), mod[:, 2].clone(), mod[:, 3].clone(), mod[:, 4].clone(), mod[:, 5].clone())
    h = _layernorm(x, axis=-1) * (1 + ca) + sa
    x = x + ga * _A_ref(h, A_k, A_v, A_shift_k, A_ssm_k, A_ssm_D)
    h = _layernorm(x, axis=-1) * (1 + cb) + sb
    x = x + gb * _B_ref(h, B_w_r, B_w_gate, B_w_up, B_w_down)
    return x
```

```cpp
#include <hip/hip_runtime.h>
#include <hip/hip_bf16.h>
#include <stdint.h>

typedef unsigned short u16;
#define S_ 2048
#define D_ 640
#define F_ 1728

__device__ __forceinline__ float bf2f(u16 v) { return __uint_as_float(((unsigned)v) << 16); }
__device__ __forceinline__ u16 f2bf(float f) { __bf16 b = (__bf16)f; return __builtin_bit_cast(u16, b); }
__device__ __forceinline__ float rb(float f) { return bf2f(f2bf(f)); }

__global__ void n_mod(const u16* c, const u16* w, float* mod) {
    int j = blockIdx.x * blockDim.x + threadIdx.x;
    if (j >= 6 * D_) return;
    float acc = 0.f;
    for (int i = 0; i < D_; ++i) {
        float cv = bf2f(c[i]);
        float s = rb(cv / (1.f + __expf(-cv)));
        acc += s * bf2f(w[(size_t)i * 6 * D_ + j]);
    }
    mod[j] = rb(acc);
}

__global__ void n_kv(const u16* k, const u16* v, const u16* sh, u16* kv) {
    int idx = blockIdx.x * blockDim.x + threadIdx.x;
    int t = idx / D_, d = idx % D_;
    float acc = 0.f;
    for (int j = 0; j < 4; ++j) {
        int s = t - 3 + j;
        float kk = s >= 0 ? bf2f(k[s * D_ + d]) : 0.f;
        acc = rb(acc + rb(kk * bf2f(sh[d * 4 + 3 - j])));
    }
    kv[idx] = f2bf(acc * bf2f(v[idx]));
}

__global__ void n_conv(const u16* kv, const u16* sk, const u16* sD, u16* y2) {
    int idx = blockIdx.x * blockDim.x + threadIdx.x;
    int t = idx / D_, d = idx % D_;
    float acc = 0.f;
    for (int s = 0; s <= t; ++s) acc += bf2f(sk[d * S_ + t - s]) * bf2f(kv[s * D_ + d]);
    float y = rb(acc);
    y2[idx] = f2bf(y + rb(bf2f(kv[idx]) * bf2f(sD[d])));
}

__device__ float block_sum(float v, float* sm) {
    for (int o = 32; o > 0; o >>= 1) v += __shfl_xor(v, o);
    __syncthreads();
    if ((threadIdx.x & 63) == 0) sm[threadIdx.x >> 6] = v;
    __syncthreads();
    float r = 0.f;
    for (int i = 0; i < (int)(blockDim.x >> 6); ++i) r += sm[i];
    return r;
}

__global__ void n_tok(const u16* x, const u16* y2, const float* mod, const u16* wr, u16* x1o, u16* h2o, float* r, u16* out) {
    __shared__ float sm[16];
    int t = blockIdx.x, d = threadIdx.x;
    float xv = bf2f(x[t * D_ + d]);
    float mf = block_sum(xv, sm) / D_;
    float var = block_sum((xv - mf) * (xv - mf), sm) / D_;
    float m = rb(mf), v = rb(var);
    float h = rb(rb(xv - m) / rb(sqrtf(rb(v + 1e-5f))));
    h = rb(rb(h * rb(1.f + mod[D_ + d])) + mod[d]);
    float a = rb(h * bf2f(y2[t * D_ + d]));
    float x1 = rb(xv + rb(mod[2 * D_ + d] * a));
    mf = block_sum(x1, sm) / D_;
    var = block_sum((x1 - mf) * (x1 - mf), sm) / D_;
    m = rb(mf); v = rb(var);
    float h2 = rb(rb(x1 - m) / rb(sqrtf(rb(v + 1e-5f))));
    h2 = rb(rb(h2 * rb(1.f + mod[4 * D_ + d])) + mod[3 * D_ + d]);
    float rr = block_sum(h2 * bf2f(wr[d]), sm);
    if (d == 0) r[t] = rb(rr);
    x1o[t * D_ + d] = f2bf(x1);
    h2o[t * D_ + d] = f2bf(h2);
    out[t * D_ + d] = f2bf(x1 + rb(mod[5 * D_ + d] * h2));
}

__global__ void n_top2(const float* r, int* idx) {
    if (threadIdx.x == 0) {
        int i0 = -1, i1 = -1; float v0 = -INFINITY, v1 = -INFINITY;
        for (int t = 0; t < S_; ++t) {
            float v = r[t];
            if (i0 < 0 || v > v0) { v1 = v0; i1 = i0; v0 = v; i0 = t; }
            else if (i1 < 0 || v > v1) { v1 = v; i1 = t; }
        }
        idx[0] = i0; idx[1] = i1;
    }
}

__global__ void n_gu(const u16* h2, const int* idx, const u16* wg, const u16* wu, float* gu) {
    int f = blockIdx.x * blockDim.x + threadIdx.x;
    int c = blockIdx.y;
    if (f >= F_) return;
    int t = idx[c];
    float g = 0.f, u = 0.f;
    for (int i = 0; i < D_; ++i) {
        float h = bf2f(h2[t * D_ + i]);
        g += h * bf2f(wg[(size_t)i * F_ + f]);
        u += h * bf2f(wu[(size_t)i * F_ + f]);
    }
    g = rb(g); u = rb(u);
    g = rb(g / (1.f + __expf(-g)));
    gu[c * F_ + f] = rb(g * u);
}

__global__ void n_down(const float* gu, const int* idx, const float* r, const u16* wd, const u16* x1, const u16* h2, const float* mod, u16* out) {
    int d = threadIdx.x;
    int c = blockIdx.x;
    int t = idx[c];
    float y = 0.f;
    for (int f = 0; f < F_; ++f) y += gu[c * F_ + f] * bf2f(wd[(size_t)f * D_ + d]);
    y = rb(y);
    float rs = rb(1.f / (1.f + __expf(-r[t])));
    y = rb(y * rs);
    float b = rb(bf2f(h2[t * D_ + d]) + y);
    out[t * D_ + d] = f2bf(bf2f(x1[t * D_ + d]) + rb(mod[5 * D_ + d] * b));
}

extern "C" void kernel_launch(void* const* d_in, const int* in_sizes, int n_in,
                              void* d_out, int out_size, void* d_ws, size_t ws_size,
                              hipStream_t stream) {
    const u16* q = (const u16*)d_in[0];
    const u16* k = (const u16*)d_in[1];
    const u16* v = (const u16*)d_in[2];
    const u16* sh = (const u16*)d_in[3];
    const u16* sk = (const u16*)d_in[4];
    const u16* sD = (const u16*)d_in[5];
    const u16* wr = (const u16*)d_in[7];
    const u16* wg = (const u16*)d_in[8];
    const u16* wu = (const u16*)d_in[9];
    const u16* wd = (const u16*)d_in[10];
    const u16* c = (const u16*)d_in[11];
    const u16* wm = (const u16*)d_in[12];
    char* ws = (char*)d_ws;
    float* mod = (float*)ws;
    float* r = (float*)(ws + 16384);
    int* idx = (int*)(ws + 32768);
    float* gu = (float*)(ws + 36864);
    u16* kv = (u16*)(ws + (1 << 20));
    u16* y2 = (u16*)(ws + (4 << 20));
    u16* x1 = (u16*)(ws + (8 << 20));
    u16* h2 = (u16*)(ws + (12 << 20));
    u16* out = (u16*)d_out;
    n_mod<<<60, 64, 0, stream>>>(c, wm, mod);
    n_kv<<<S_ * D_ / 256, 256, 0, stream>>>(k, v, sh, kv);
    n_conv<<<S_ * D_ / 256, 256, 0, stream>>>(kv, sk, sD, y2);
    n_tok<<<S_, D_, 0, stream>>>(q, y2, mod, wr, x1, h2, r, out);
    n_top2<<<1, 64, 0, stream>>>(r, idx);
    n_gu<<<dim3(27, 2), 64, 0, stream>>>(h2, idx, wg, wu, gu);
    n_down<<<2, D_, 0, stream>>>(gu, idx, r, wd, x1, h2, mod, out);
}
```

```cpp
#include <hip/hip_runtime.h>
#include <stdint.h>

typedef unsigned short u16;
typedef unsigned u32;
typedef unsigned long long u64;
typedef u32 v2u __attribute__((ext_vector_type(2)));
typedef u32 v4u __attribute__((ext_vector_type(4)));
typedef float f2 __attribute__((ext_vector_type(2)));
typedef float f4 __attribute__((ext_vector_type(4)));
typedef __bf16 bf2 __attribute__((ext_vector_type(2)));
typedef __bf16 bf8 __attribute__((ext_vector_type(8)));
typedef short s4 __attribute__((ext_vector_type(4)));
#define LAS __attribute__((address_space(3)))
#define GAS __attribute__((address_space(1)))
#define RLX __ATOMIC_RELAXED, __HIP_MEMORY_SCOPE_AGENT
#define SC1 16
#define POISON 0xFFFFFFFFu
#define OUT_AUX 16

constexpr int S_ = 2048, D_ = 640, F_ = 1728;
constexpr u32 W_EPOCH = 0;
constexpr u32 W_HINT = 1024;
constexpr u32 W_FLAGA = 4096;
constexpr u32 W_FLAGP = 8192;
constexpr u32 W_FLAGM = 12288;
constexpr u32 W_MOD = 16384;
constexpr u32 W_R = 32768;
constexpr u32 W_A = 49152;
constexpr u32 W_ROWS = 65536;
constexpr u32 W_KVT = 2u << 20;
constexpr u32 YS = 10240;
constexpr u32 W_Y = 5u << 20;
constexpr u32 W_END = 8u << 20;
constexpr u32 W_STAMP = 16u << 20;
constexpr u32 CHS = 37952, KCS = 4160, L_R = 33280;
constexpr u32 L_CTL = 4 * CHS;
constexpr u32 L_LN1 = L_CTL + 64;
constexpr u32 L_CC = L_LN1 + 512;
constexpr u32 L_P2 = L_CC + 64;
constexpr u32 L_T2 = L_P2 + 512;
constexpr u32 L_RED = L_T2 + 64;
constexpr u32 L_MODP = L_RED + 2048;
constexpr u32 L_END = L_MODP + 1024;

#define DLY_A 30
#define DLY_Y 60
#define DLY_R 40
#define DLY_D 80

struct Params {
    const u16 *x, *k, *v, *sh, *sk, *sD, *wr, *wg, *wu, *wd, *c, *wm;
    u16* out;
    char* ws;
};

__device__ __forceinline__ float bflo(u32 w) { return __uint_as_float(w << 16); }
__device__ __forceinline__ float bfhi(u32 w) { return __uint_as_float(w & 0xFFFF0000u); }
__device__ __forceinline__ f2 bfup(u32 w) { f2 r = {bflo(w), bfhi(w)}; return r; }
__device__ __forceinline__ u32 pk(float lo, float hi) { f2 a = {lo, hi}; return __builtin_bit_cast(u32, __builtin_convertvector(a, bf2)); }
__device__ __forceinline__ u32 pk2(f2 a) { return __builtin_bit_cast(u32, __builtin_convertvector(a, bf2)); }
__device__ __forceinline__ u32 san(u32 w) { return w < 0xFFFFFFFEu ? w : 0xFFFFFFFEu; }
__device__ __forceinline__ u32 umax(u32 a, u32 b) { return a > b ? a : b; }
__device__ __forceinline__ u32 umin(u32 a, u32 b) { return a < b ? a : b; }
__device__ __forceinline__ bool clean4(v4u d) { return umax(umax(d.x, d.y), umax(d.z, d.w)) != POISON; }
__device__ __forceinline__ u64 now() { return __builtin_amdgcn_s_memrealtime(); }
template <int C> __device__ __forceinline__ u32 dppu(u32 x) { return (u32)__builtin_amdgcn_mov_dpp((int)x, C, 0xf, 0xf, true); }
template <int C> __device__ __forceinline__ float dppf(float x) { return __uint_as_float(dppu<C>(__float_as_uint(x))); }
constexpr int DPP_X1 = 0xB1, DPP_X2 = 0x4E, DPP_HM = 0x141, DPP_M = 0x140, DPP_ROR4 = 0x124, DPP_ROR8 = 0x128;
__device__ __forceinline__ float add32(float a, float b) {
    auto s = __builtin_amdgcn_permlane32_swap(__float_as_uint(a), __float_as_uint(b), false, false);
    return __uint_as_float(s[0]) + __uint_as_float(s[1]);
}
__device__ __forceinline__ float add16(float a, float b) {
    auto s = __builtin_amdgcn_permlane16_swap(__float_as_uint(a), __float_as_uint(b), false, false);
    return __uint_as_float(s[0]) + __uint_as_float(s[1]);
}
__device__ __forceinline__ void top2_merge(u32& a1, u32& a2, u32 b1, u32 b2) {
    u32 t = umin(a1, b1), u = umax(a2, b2);
    a1 = umax(a1, b1); a2 = umax(t, u);
}
__device__ __forceinline__ void wait_until(u64 t) { while (now() < t) __builtin_amdgcn_s_sleep(1); }
__device__ __forceinline__ void bar() { asm volatile("s_waitcnt lgkmcnt(0)\n\ts_barrier" ::: "memory"); }
#define DRAIN() asm volatile("s_waitcnt vmcnt(0)" ::: "memory")
#define CFENCE() asm volatile("" ::: "memory")
#define TMO 300000ull

#define STAMP(i) do {} while (0)

__global__ void __launch_bounds__(576) fused_kernel(Params p) {
    __shared__ __attribute__((aligned(16))) char smem[L_END];
    LAS char* lds = (LAS char*)smem;
    const int tid = threadIdx.x, b = blockIdx.x, lane = tid & 63;
    const int wave = __builtin_amdgcn_readfirstlane(tid >> 6);
    const u64 tstart = now(), tmo = tstart + TMO;
    const __amdgpu_buffer_rsrc_t wr = __builtin_amdgcn_make_buffer_rsrc(p.ws, 0, W_END, 0x00020000);
    GAS u64* const wsq = (GAS u64*)p.ws;
    LAS u32* const ctl = (LAS u32*)(lds + L_CTL);

    if (wave < 5) {
        const int tt = 4 * (b & 7) + ((b >> 3) & 3), cg = b >> 5;
        const int t0 = 64 * tt + 8 * (tid & 7), ch = 80 * cg + 2 * (tid >> 3);
        u32 kw[11], vw[8];
#pragma unroll
        for (int i = 0; i < 11; ++i) { int t = t0 - 3 + i; kw[i] = *(const u32*)(p.k + (t < 0 ? 0 : t) * D_ + ch); }
#pragma unroll
        for (int i = 0; i < 8; ++i) vw[i] = *(const u32*)(p.v + (t0 + i) * D_ + ch);
        const v4u sw = *(const v4u*)(p.sh + ch * 4);
        const u64 e0 = __hip_atomic_load(wsq + W_EPOCH / 8, RLX);
        if (tid < 16) ctl[tid] = 0;
        bar();
        const u64 epoch = (((u64)__builtin_amdgcn_readfirstlane((u32)(e0 >> 32)) << 32) | (u32)__builtin_amdgcn_readfirstlane((u32)e0)) * 0xD1342543DE82EF95ull + 0x9E3779B97F4A7C15ull;
        {
            if (t0 == 0) { kw[0] = 0; kw[1] = 0; kw[2] = 0; }
            const f2 s0 = {bflo(sw.x), bflo(sw.z)}, s1 = {bfhi(sw.x), bfhi(sw.z)}, s2 = {bflo(sw.y), bflo(sw.w)}, s3 = {bfhi(sw.y), bfhi(sw.w)};
            f2 kf[11], u[8];
#pragma unroll
            for (int i = 0; i < 11; ++i) kf[i] = bfup(kw[i]);
#pragma unroll
            for (int i = 0; i < 8; ++i) u[i] = (kf[i] * s3 + kf[i + 1] * s2 + kf[i + 2] * s1 + kf[i + 3] * s0) * bfup(vw[i]);
            STAMP(1);
            v4u c0 = {pk(u[7].x, u[6].x), pk(u[5].x, u[4].x), pk(u[3].x, u[2].x), pk(u[1].x, u[0].x)};
            v4u c1 = {pk(u[7].y, u[6].y), pk(u[5].y, u[4].y), pk(u[3].y, u[2].y), pk(u[1].y, u[0].y)};
            const u32 off = W_KVT + ch * 4096 + (2040 - t0) * 2;
            __builtin_amdgcn_raw_buffer_store_b128(c0, wr, off, 0, SC1);
            __builtin_amdgcn_raw_buffer_store_b128(c1, wr, off + 4096, 0, SC1);
        }
        DRAIN();
        STAMP(2);
        if (lane == 0) {
            u32 old = __hip_atomic_fetch_add(ctl + 0, 1u, __ATOMIC_RELAXED, __HIP_MEMORY_SCOPE_WORKGROUP);
            if (old == 4) __hip_atomic_store(wsq + W_FLAGA / 8 + cg * 32 + tt, epoch, RLX);
        }
        const int tp = lane & 3, cb = lane >> 2;
        const int tokE = 8 * b + 2 * tp;
        v2u xw[2][2];
#pragma unroll
        for (int i = 0; i < 2; ++i)
#pragma unroll
            for (int o = 0; o < 2; ++o) xw[o][i] = *(const v2u*)(p.x + (tokE + o) * D_ + 64 * (2 * wave + i) + 4 * cb);
        v2u wrw[2];
#pragma unroll
        for (int i = 0; i < 2; ++i) wrw[i] = *(const v2u*)(p.wr + 64 * (2 * wave + i) + 4 * cb);
        {
            const v4u ps = {POISON, POISON, POISON, POISON};
            __builtin_amdgcn_raw_buffer_store_b128(ps, wr, W_Y + b * YS + tid * 16, 0, SC1);
            __builtin_amdgcn_raw_buffer_store_b128(ps, wr, W_Y + b * YS + 5120 + tid * 16, 0, SC1);
            __builtin_amdgcn_raw_buffer_store_b128(ps, wr, W_ROWS + b * 5120 + tid * 16, 0, SC1);
            if (tid < 2) __builtin_amdgcn_raw_buffer_store_b128(ps, wr, W_R + b * 32 + tid * 16, 0, SC1);
            if (b >= 160 && b < 214 && tid >= 64 && tid < 72) __builtin_amdgcn_raw_buffer_store_b128(ps, wr, W_A + (b - 160) * 128 + (tid - 64) * 16, 0, SC1);
        }
        DRAIN();
        if (lane == 0) {
            u32 old = __hip_atomic_fetch_add(ctl + 1, 1u, __ATOMIC_RELAXED, __HIP_MEMORY_SCOPE_WORKGROUP);
            if (old == 4) __hip_atomic_store(wsq + W_FLAGP / 8 + b, epoch, RLX);
        }
        STAMP(3);
        f2 xf[2][4];
#pragma unroll
        for (int o = 0; o < 2; ++o) { xf[o][0] = bfup(xw[o][0].x); xf[o][1] = bfup(xw[o][0].y); xf[o][2] = bfup(xw[o][1].x); xf[o][3] = bfup(xw[o][1].y); }
        const int tl = 2 * (lane & 3) + ((lane >> 2) & 1);
        float mE, mO, rE, rO;
        {
            float s1[2], s2[2];
#pragma unroll
            for (int o = 0; o < 2; ++o) {
                f2 a = (xf[o][0] + xf[o][1]) + (xf[o][2] + xf[o][3]);
                f2 q = xf[o][0] * xf[o][0] + xf[o][1] * xf[o][1] + xf[o][2] * xf[o][2] + xf[o][3] * xf[o][3];
                s1[o] = a.x + a.y; s2[o] = q.x + q.y;
            }
            float q12 = add16(add32(s1[0], s1[1]), add32(s2[0], s2[1]));
            q12 += dppf<DPP_ROR8>(q12); q12 += dppf<DPP_ROR4>(q12);
            if ((lane & 12) == 0) *(LAS float*)(lds + L_LN1 + (2 * tp + (lane >> 5)) * 64 + (((lane >> 4) & 1) * 8 + wave) * 4) = q12;
            asm volatile("s_waitcnt lgkmcnt(0)" ::: "memory");
            if (lane == 0) __hip_atomic_fetch_add(ctl + 2, 1u, __ATOMIC_RELAXED, __HIP_MEMORY_SCOPE_WORKGROUP);
            while (__hip_atomic_load(ctl + 2, __ATOMIC_RELAXED, __HIP_MEMORY_SCOPE_WORKGROUP) < 5u) __builtin_amdgcn_s_sleep(1);
            CFENCE();
            const LAS float* q = (const LAS float*)(lds + L_LN1 + tl * 64);
            float a = ((q[0] + q[1]) + (q[2] + q[3])) + q[4], c = ((q[8] + q[9]) + (q[10] + q[11])) + q[12];
            float m = a * (1.f / D_), rs = __builtin_amdgcn_rsqf(fmaxf(c * (1.f / D_) - m * m, 0.f) + 1e-5f);
            float m2 = dppf<DPP_ROR4>(m), rs2 = dppf<DPP_ROR4>(rs);
            const bool od = (lane >> 2) & 1;
            mE = od ? m2 : m; mO = od ? m : m2; rE = od ? rs2 : rs; rO = od ? rs : rs2;
        }
        {
            const int f = lane < 60 ? lane : 0;
            while (!__all(__hip_atomic_load(wsq + W_FLAGM / 8 + f, RLX) == epoch) && now() < tmo) __builtin_amdgcn_s_sleep(8);
            CFENCE();
        }
        STAMP(4);
        f2 gh[2][4], cw[4], c1b[4], sb[4], gb[4];
        float C1, C2;
        {
            v2u mw[6][2];
#pragma unroll
            for (int j = 0; j < 6; ++j)
#pragma unroll
                for (int i = 0; i < 2; ++i) mw[j][i] = __builtin_amdgcn_raw_buffer_load_b64(wr, W_MOD + (j * D_ + 64 * (2 * wave + i) + 4 * cb) * 2, 0, SC1);
            f2 c1 = {0.f, 0.f}, c2 = {0.f, 0.f};
#pragma unroll
            for (int j = 0; j < 4; ++j) {
                const int i = j >> 1;
#define PICK(a) ((j & 1) ? (a)[i].y : (a)[i].x)
                const f2 sa = bfup(PICK(mw[0])), ca = bfup(PICK(mw[1])) + 1.f, ga = bfup(PICK(mw[2])), w = bfup(PICK(wrw));
                sb[j] = bfup(PICK(mw[3])); c1b[j] = bfup(PICK(mw[4])) + 1.f; gb[j] = bfup(PICK(mw[5]));
#undef PICK
                gh[0][j] = ga * ((xf[0][j] - mE) * rE * ca + sa);
                gh[1][j] = ga * ((xf[1][j] - mO) * rO * ca + sa);
                cw[j] = c1b[j] * w;
                c1 += cw[j]; c2 += sb[j] * w;
            }
            float q = add32(c1.x + c1.y, c2.x + c2.y);
            q = add16(q, q);
            q += dppf<DPP_ROR8>(q); q += dppf<DPP_ROR4>(q);
            if ((lane & 31) == 0) *(LAS float*)(lds + L_CC + (lane >> 5) * 32 + wave * 4) = q;
            asm volatile("s_waitcnt lgkmcnt(0)" ::: "memory");
        }
        STAMP(5);
        bar();
        {
            const LAS float* q = (const LAS float*)(lds + L_CC);
            C1 = ((q[0] + q[1]) + (q[2] + q[3])) + q[4]; C2 = ((q[8] + q[9]) + (q[10] + q[11])) + q[12];
        }
        if (b >= 160) { while (__hip_atomic_load(wsq + W_HINT / 8, RLX) != epoch && now() < tmo) __builtin_amdgcn_s_sleep(4); wait_until(now() + DLY_Y - 30); }
        else wait_until(now() + DLY_Y);
        STAMP(6);
        v4u yw[2];
        for (;;) {
            CFENCE();
            yw[0] = __builtin_amdgcn_raw_buffer_load_b128(wr, W_Y + b * YS + wave * 2048 + lane * 16, 0, SC1);
            yw[1] = __builtin_amdgcn_raw_buffer_load_b128(wr, W_Y + b * YS + wave * 2048 + 1024 + lane * 16, 0, SC1);
            if (__all(clean4(yw[0]) && clean4(yw[1])) || now() > tmo) break;
        }
        STAMP(7);
        f2 x1[2][4];
        x1[0][0] = gh[0][0] * bfup(yw[0].x) + xf[0][0]; x1[0][1] = gh[0][1] * bfup(yw[0].y) + xf[0][1];
        x1[1][0] = gh[1][0] * bfup(yw[0].z) + xf[1][0]; x1[1][1] = gh[1][1] * bfup(yw[0].w) + xf[1][1];
        x1[0][2] = gh[0][2] * bfup(yw[1].x) + xf[0][2]; x1[0][3] = gh[0][3] * bfup(yw[1].y) + xf[0][3];
        x1[1][2] = gh[1][2] * bfup(yw[1].z) + xf[1][2]; x1[1][3] = gh[1][3] * bfup(yw[1].w) + xf[1][3];
        {
            float s1[2], s2[2], s3[2];
#pragma unroll
            for (int o = 0; o < 2; ++o) {
                f2 a = (x1[o][0] + x1[o][1]) + (x1[o][2] + x1[o][3]);
                f2 q = x1[o][0] * x1[o][0] + x1[o][1] * x1[o][1] + x1[o][2] * x1[o][2] + x1[o][3] * x1[o][3];
                f2 r = x1[o][0] * cw[0] + x1[o][1] * cw[1] + x1[o][2] * cw[2] + x1[o][3] * cw[3];
                s1[o] = a.x + a.y; s2[o] = q.x + q.y; s3[o] = r.x + r.y;
            }
            float q12 = add16(add32(s1[0], s1[1]), add32(s2[0], s2[1]));
            float q3 = add32(s3[0], s3[1]); q3 = add16(q3, q3);
            q12 += dppf<DPP_ROR8>(q12); q3 += dppf<DPP_ROR8>(q3);
            q12 += dppf<DPP_ROR4>(q12); q3 += dppf<DPP_ROR4>(q3);
            if ((lane & 12) == 0) {
                LAS float* q = (LAS float*)(lds + L_P2 + (2 * tp + (lane >> 5)) * 64);
                q[((lane >> 4) & 1) * 5 + wave] = q12;
                q[10 + wave] = q3;
            }
        }
        bar();
        u32 keyE, keyO, k1, k2;
        {
            const LAS f4* q = (const LAS f4*)(lds + L_P2 + tl * 64);
            const f4 a = q[0], c = q[1], d = q[2], e = q[3];
            const float S1 = ((a.x + a.y) + (a.z + a.w)) + c.x, S2 = ((c.y + c.z) + (c.w + d.x)) + d.y, S3 = ((d.z + d.w) + (e.x + e.y)) + e.z;
            const float m = S1 * (1.f / D_), rs = __builtin_amdgcn_rsqf(fmaxf(S2 * (1.f / D_) - m * m, 0.f) + 1e-5f);
            const float r = rs * (S3 - m * C1) + C2;
            u32 u = __float_as_uint(r);
            u = (u & 0x80000000u) ? ~u : (u | 0x80000000u);
            const u32 key = san((u & ~0x7FFu) | (u32)(2047 - (8 * b + tl)));
            if (tid < 8) __builtin_amdgcn_raw_buffer_store_b32(key, wr, W_R + (8 * b + tl) * 4, 0, SC1);
            STAMP(8);
            const float m2 = dppf<DPP_ROR4>(m), rs2 = dppf<DPP_ROR4>(rs);
            const u32 key2 = dppu<DPP_ROR4>(key);
            const bool od = (lane >> 2) & 1;
            mE = od ? m2 : m; mO = od ? m : m2; rE = od ? rs2 : rs; rO = od ? rs : rs2;
            keyE = od ? key2 : key; keyO = od ? key : key2;
            k1 = umax(key, key2); k2 = umin(key, key2);
            top2_merge(k1, k2, dppu<DPP_X1>(k1), dppu<DPP_X1>(k2));
            top2_merge(k1, k2, dppu<DPP_X2>(k1), dppu<DPP_X2>(k2));
        }
        u32 ow[2][4];
#pragma unroll
        for (int o = 0; o < 2; ++o) {
            const float m = o ? mO : mE, rs = o ? rO : rE;
            const u32 key = o ? keyO : keyE;
            u32 hw[4], xw1[4];
#pragma unroll
            for (int j = 0; j < 4; ++j) {
                const f2 h = (x1[o][j] - m) * rs * c1b[j] + sb[j];
                ow[o][j] = pk2(gb[j] * h + x1[o][j]);
                hw[j] = san(pk2(h)); xw1[j] = san(pk2(x1[o][j]));
            }
            if (key == k1 || key == k2) {
                const u32 off = W_ROWS + b * 5120 + (key == k1 ? 0 : 2560) + (128 * wave + 4 * cb) * 2;
                v2u a = {hw[0], hw[1]}, c = {hw[2], hw[3]}, d = {xw1[0], xw1[1]}, e = {xw1[2], xw1[3]};
                __builtin_amdgcn_raw_buffer_store_b64(a, wr, off, 0, SC1);
                __builtin_amdgcn_raw_buffer_store_b64(c, wr, off + 128, 0, SC1);
                __builtin_amdgcn_raw_buffer_store_b64(d, wr, off + 1280, 0, SC1);
                __builtin_amdgcn_raw_buffer_store_b64(e, wr, off + 1280 + 128, 0, SC1);
            }
        }
        STAMP(9);
        bar();
        {
            const LAS v4u* q = (const LAS v4u*)(lds + L_T2);
            const v4u a = q[0], c = q[1];
            u32 g1 = a.x, g2 = a.y;
            top2_merge(g1, g2, a.z, a.w); top2_merge(g1, g2, c.x, c.y); top2_merge(g1, g2, c.z, c.w);
            const __amdgpu_buffer_rsrc_t orr = __builtin_amdgcn_make_buffer_rsrc(p.out, 0, S_ * D_ * 2, 0x00020000);
#pragma unroll
            for (int o = 0; o < 2; ++o) {
                const u32 key = o ? keyO : keyE;
                if (key != g1 && key != g2) {
                    const u32 off = ((tokE + o) * D_ + 128 * wave + 4 * cb) * 2;
                    v2u a0 = {ow[o][0], ow[o][1]}, a1 = {ow[o][2], ow[o][3]};
                    __builtin_amdgcn_raw_buffer_store_b64(a0, orr, off, 0, OUT_AUX);
                    __builtin_amdgcn_raw_buffer_store_b64(a1, orr, off + 128, 0, OUT_AUX);
                }
            }
        }
        STAMP(10);
        return;
    }

    const int w = wave - 5, ct = tid - 320;
    const int col = lane & 15, q4 = lane >> 4;
    const int ab = b - 160;
    const bool isP3 = ab >= 0 && ab < 54, isP4 = ab >= 54 && ab < 94;
    u64 epoch;
    u32 g1, g2;
    auto top2 = [&]() __attribute__((always_inline)) {
    bar();
    wait_until(now() + DLY_R);
    STAMP(7);
    {
        v4u a, c;
        for (;;) {
            CFENCE();
            a = __builtin_amdgcn_raw_buffer_load_b128(wr, W_R + ct * 16, 0, SC1);
            c = __builtin_amdgcn_raw_buffer_load_b128(wr, W_R + 4096 + ct * 16, 0, SC1);
            if (__all(clean4(a) && clean4(c)) || now() > tmo) break;
        }
        STAMP(8);
        g1 = umax(a.x, a.y); g2 = umin(a.x, a.y);
        top2_merge(g1, g2, umax(a.z, a.w), umin(a.z, a.w));
        u32 h1 = umax(c.x, c.y), h2 = umin(c.x, c.y);
        top2_merge(h1, h2, umax(c.z, c.w), umin(c.z, c.w));
        top2_merge(g1, g2, h1, h2);
        top2_merge(g1, g2, dppu<DPP_X1>(g1), dppu<DPP_X1>(g2));
        top2_merge(g1, g2, dppu<DPP_X2>(g1), dppu<DPP_X2>(g2));
        top2_merge(g1, g2, dppu<DPP_HM>(g1), dppu<DPP_HM>(g2));
        top2_merge(g1, g2, dppu<DPP_M>(g1), dppu<DPP_M>(g2));
        { auto s = __builtin_amdgcn_permlane16_swap(g1, g1, false, false); auto t = __builtin_amdgcn_permlane16_swap(g2, g2, false, false);
          g1 = s[0]; g2 = t[0]; top2_merge(g1, g2, s[1], t[1]); }
        { auto s = __builtin_amdgcn_permlane32_swap(g1, g1, false, false); auto t = __builtin_amdgcn_permlane32_swap(g2, g2, false, false);
          g1 = s[0]; g2 = t[0]; top2_merge(g1, g2, s[1], t[1]); }
        if (lane == 0) { v2u g = {g1, g2}; *(LAS v2u*)(lds + L_T2 + w * 8) = g; }
    }
    bar();
        const LAS v4u* q = (const LAS v4u*)(lds + L_T2);
        const v4u a = q[0], c = q[1];
        g1 = a.x; g2 = a.y;
        top2_merge(g1, g2, a.z, a.w); top2_merge(g1, g2, c.x, c.y); top2_merge(g1, g2, c.z, c.w);
        STAMP(9);
    };
    auto waitP = [&]() __attribute__((always_inline)) {
        STAMP(2);
        for (;;) {
            bool ok = true;
#pragma unroll
            for (int j = 0; j < 4; ++j) ok &= __hip_atomic_load(wsq + W_FLAGP / 8 + 64 * j + lane, RLX) == epoch;
            if (__all(ok) || now() > tmo) break;
            __builtin_amdgcn_s_sleep(8);
        }
        STAMP(3);
        bar();
    };
#define WINNERS() const int t1 = 2047 - (g1 & 0x7FF), t2 = 2047 - (g2 & 0x7FF); \
    const u32 row1 = W_ROWS + (t1 >> 3) * 5120, row2 = W_ROWS + (t2 >> 3) * 5120 + ((t1 >> 3) == (t2 >> 3) ? 2560 : 0); \
    const v4u z4 = {0, 0, 0, 0};
    if (b < 160) {
        const int ch = 4 * b + w;
        LAS char* cbase = lds + w * CHS;
        v4u c0[4], c1[4];
#pragma unroll
        for (int j = 0; j < 4; ++j) {
            const int i = 64 * j + lane;
            c1[j] = *(const v4u*)(p.sk + ch * S_ + 8 * i);
            c0[j] = *(const v4u*)(p.sk + ch * S_ + 8 * (i > 0 ? i - 1 : 0));
        }
        const u32 db = p.sD[ch];
        const u64 e0 = __hip_atomic_load(wsq + W_EPOCH / 8, RLX);
        bar();
        epoch = (((u64)__builtin_amdgcn_readfirstlane((u32)(e0 >> 32)) << 32) | (u32)__builtin_amdgcn_readfirstlane((u32)e0)) * 0xD1342543DE82EF95ull + 0x9E3779B97F4A7C15ull;
        const v4u z4 = {0, 0, 0, 0};
        if (lane == 0) c0[0] = z4;
        if (lane < 8) *(LAS v4u*)(cbase + lane * KCS) = z4;
        if (lane < 30) *(LAS v4u*)(cbase + L_R + 4096 + lane * 16) = z4;
#pragma unroll
        for (int j = 0; j < 4; ++j) {
            const u32 e[8] = {c0[j].x, c0[j].y, c0[j].z, c0[j].w, c1[j].x, c1[j].y, c1[j].z, c1[j].w};
            u32 o[7];
#pragma unroll
            for (int i = 0; i < 7; ++i) o[i] = __builtin_amdgcn_alignbit(e[i + 1], e[i], 16);
            LAS char* dst = cbase + (64 * j + lane + 1) * 16;
#pragma unroll
            for (int m = 0; m < 4; ++m) {
                const v4u a = {o[m], o[m + 1], o[m + 2], o[m + 3]}, c = {e[m + 1], e[m + 2], e[m + 3], e[m + 4]};
                *(LAS v4u*)(dst + (2 * m) * KCS) = a;
                *(LAS v4u*)(dst + (2 * m + 1) * KCS) = c;
            }
        }
        bf8 AD;
        {
            const int js = 15 - col - 8 * q4;
            v4u d;
            d.x = js == 0 ? db : js == 1 ? db << 16 : 0u; d.y = js == 2 ? db : js == 3 ? db << 16 : 0u;
            d.z = js == 4 ? db : js == 5 ? db << 16 : 0u; d.w = js == 6 ? db : js == 7 ? db << 16 : 0u;
            AD = __builtin_bit_cast(bf8, d);
        }
        STAMP(1);
        while (__hip_atomic_load(ctl + 0, __ATOMIC_RELAXED, __HIP_MEMORY_SCOPE_WORKGROUP) < 5u) __builtin_amdgcn_s_sleep(1);
        wait_until(now() + DLY_A);
        STAMP(2);
        {
            const int f = (ch / 80) * 32 + (lane & 31);
            while (!__all(__hip_atomic_load(wsq + W_FLAGA / 8 + f, RLX) == epoch) && now() < tmo) {}
            CFENCE();
        }
        STAMP(3);
        {
            v4u r[4];
#pragma unroll
            for (int j = 0; j < 4; ++j) r[j] = __builtin_amdgcn_raw_buffer_load_b128(wr, W_KVT + ch * 4096 + j * 1024 + lane * 16, 0, SC1);
#pragma unroll
            for (int j = 0; j < 4; ++j) *(LAS v4u*)(cbase + L_R + j * 1024 + lane * 16) = r[j];
        }
        STAMP(4);
        u64 fp[4];
#pragma unroll
        for (int j = 0; j < 4; ++j) fp[j] = __hip_atomic_load(wsq + W_FLAGP / 8 + 64 * j + lane, RLX);
        f4 acc[8];
#pragma unroll
        for (int i = 0; i < 8; ++i) acc[i] = (f4){0.f, 0.f, 0.f, 0.f};
        const LAS char* pa = cbase + (col & 7) * KCS + (q4 + (col >> 3)) * 16;
        const LAS char* pb = cbase + L_R + 480 + 16 * q4 - 32 * col;
#define LDA(a, r) (*(const LAS bf8*)(pa + (a) * 512 + (r) * 64))
#define LDB(bb, r) (*(const LAS bf8*)(pb + 3584 - (bb) * 512 + (r) * 64))
#define MM(a, bb) acc[(a) + (bb)] = __builtin_amdgcn_mfma_f32_16x16x32_bf16(A[a], B[bb], acc[(a) + (bb)], 0, 0, 0)
        __builtin_amdgcn_s_setprio(1);
#pragma unroll
        for (int r = 0; r < 8; ++r) {
            bf8 A[8], B[8];
#pragma unroll
            for (int j = 0; j < 8; ++j) { A[j] = LDA(j, r); B[j] = LDB(j, r); }
#pragma unroll
            for (int j = 0; j < 8; ++j) {
#pragma unroll
                for (int i = 0; i < j; ++i) if (i + j < 8) { MM(j, i); MM(i, j); }
                if (j < 4) MM(j, j);
                if (r == 0) acc[j] = __builtin_amdgcn_mfma_f32_16x16x32_bf16(AD, B[j], acc[j], 0, 0, 0);
            }
        }
        __builtin_amdgcn_s_setprio(0);
        STAMP(5);
        if (b == 0 && ct == 0) __hip_atomic_store(wsq + W_HINT / 8, epoch, RLX);
#pragma unroll
        for (int i = 0; i < 8; ++i) {
            LAS u32* d = (LAS u32*)(cbase + (64 * i + 4 * col + q4) * 4);
            d[0] = pk(acc[i].x, acc[i].z);
            d[512] = pk(acc[i].y, acc[i].w);
        }
        while (!__all(fp[0] == epoch && fp[1] == epoch && fp[2] == epoch && fp[3] == epoch) && now() < tmo) {
#pragma unroll
            for (int j = 0; j < 4; ++j) fp[j] = __hip_atomic_load(wsq + W_FLAGP / 8 + 64 * j + lane, RLX);
        }
        bar();
        {
            const LAS char* tb = lds + (col >> 2) * CHS + (16 * q4 + 4 * (col & 3)) * 2;
#pragma unroll
            for (int it = 0; it < 4; ++it) {
                const int P0 = (256 * w + 64 * it + 64 * b) & 1023;
                const s4 e = __builtin_amdgcn_ds_read_tr16_b64_v4i16((LAS s4*)(tb + P0 * 2));
                const s4 o = __builtin_amdgcn_ds_read_tr16_b64_v4i16((LAS s4*)(tb + P0 * 2 + 2048));
                const v2u e2 = __builtin_bit_cast(v2u, e), o2 = __builtin_bit_cast(v2u, o);
                const v4u y = {san(e2.x), san(e2.y), san(o2.x), san(o2.y)};
                const int P = P0 + lane;
                __builtin_amdgcn_raw_buffer_store_b128(y, wr, W_Y + (P >> 2) * YS + b * 64 + (P & 3) * 16, 0, SC1);
            }
        }
        STAMP(6);
        if (b == 0 && ct == 0) __hip_atomic_store(wsq + W_EPOCH / 8, epoch, RLX);
        top2();
    } else {
        const int jm = ab - 36;
        v4u mw[20]; u32 cw[20];
        if (jm >= 0) {
#pragma unroll
            for (int i = 0; i < 20; ++i) {
                const int row = 32 * i + (ct >> 3);
                mw[i] = *(const v4u*)(p.wm + row * (6 * D_) + 64 * jm + 8 * (ct & 7));
                cw[i] = p.c[row];
            }
        }
        const u64 e0 = __hip_atomic_load(wsq + W_EPOCH / 8, RLX);
        bar();
        epoch = (((u64)__builtin_amdgcn_readfirstlane((u32)(e0 >> 32)) << 32) | (u32)__builtin_amdgcn_readfirstlane((u32)e0)) * 0xD1342543DE82EF95ull + 0x9E3779B97F4A7C15ull;
        if (jm >= 0) {
            f2 a[4] = {{0.f, 0.f}, {0.f, 0.f}, {0.f, 0.f}, {0.f, 0.f}};
#pragma unroll
            for (int i = 0; i < 20; ++i) {
                const float cv = bflo(cw[i]);
                const float s = bflo(pk(cv * __builtin_amdgcn_rcpf(1.f + __expf(-cv)), 0.f));
                a[0] += bfup(mw[i].x) * s; a[1] += bfup(mw[i].y) * s; a[2] += bfup(mw[i].z) * s; a[3] += bfup(mw[i].w) * s;
            }
            float r0 = add16(add32(a[0].x, a[2].x), add32(a[1].x, a[3].x)), r1 = add16(add32(a[0].y, a[2].y), add32(a[1].y, a[3].y));
            r0 += dppf<DPP_ROR8>(r0); r1 += dppf<DPP_ROR8>(r1);
            if ((lane & 8) == 0) { f2 r = {r0, r1}; *(LAS f2*)(lds + L_MODP + w * 256 + (8 * (lane & 7) + 2 * q4) * 4) = r; }
            asm volatile("s_waitcnt lgkmcnt(0)" ::: "memory");
            u32 old = 0;
            if (lane == 0) old = __hip_atomic_fetch_add(ctl + 4, 1u, __ATOMIC_RELAXED, __HIP_MEMORY_SCOPE_WORKGROUP);
            if (__builtin_amdgcn_readfirstlane(old) == 3) {
                CFENCE();
                if (lane < 32) {
                    const LAS f2* q = (const LAS f2*)(lds + L_MODP) + lane;
                    __builtin_amdgcn_raw_buffer_store_b32(pk2((q[0] + q[32]) + (q[64] + q[96])), wr, W_MOD + jm * 128 + lane * 4, 0, SC1);
                }
                DRAIN();
                if (lane == 0) __hip_atomic_store(wsq + W_FLAGM / 8 + jm, epoch, RLX);
            }
        }
        STAMP(1);
        if (isP3) {
            bf8 wf[20];
#pragma unroll
            for (int i = 0; i < 20; ++i) {
                const int row = 160 * w + 8 * i + (lane >> 3), sg = lane & 7;
                const u16* src = (sg < 4 ? p.wg : p.wu) + row * F_ + 32 * ab + 8 * (sg & 3);
                __builtin_amdgcn_global_load_lds((const GAS void*)src, (LAS void*)(lds + w * 20480 + i * 1024), 16, 0, 0);
            }
            DRAIN();
            const LAS char* tb = lds + w * 20480 + (8 * q4 + (col >> 2)) * 128 + (col & 3) * 8;
#pragma unroll
            for (int ks = 0; ks < 5; ++ks)
#pragma unroll
                for (int tl = 0; tl < 4; ++tl) {
                    const s4 lo = __builtin_amdgcn_ds_read_tr16_b64_v4i16((LAS s4*)(tb + ks * 4096 + tl * 32));
                    const s4 hi = __builtin_amdgcn_ds_read_tr16_b64_v4i16((LAS s4*)(tb + ks * 4096 + tl * 32 + 512));
                    const v2u l2 = __builtin_bit_cast(v2u, lo), h2 = __builtin_bit_cast(v2u, hi);
                    const v4u f = {l2.x, l2.y, h2.x, h2.y};
                    wf[ks * 4 + tl] = __builtin_bit_cast(bf8, f);
                }
            waitP(); top2();
            WINNERS();
            v4u hb[5];
            const u32 off = (col == 0 ? row1 : row2) + (160 * w + 8 * q4) * 2;
            for (;;) {
                CFENCE();
                bool ok = true;
#pragma unroll
                for (int i = 0; i < 5; ++i) {
                    hb[i] = z4;
                    if (col < 2) hb[i] = __builtin_amdgcn_raw_buffer_load_b128(wr, off + i * 64, 0, SC1);
                }
#pragma unroll
                for (int i = 0; i < 5; ++i) ok &= clean4(hb[i]);
                if (__all(ok) || now() > tmo) break;
            }
            STAMP(10);
            f4 acc[4];
#pragma unroll
            for (int tl = 0; tl < 4; ++tl) {
                acc[tl] = (f4){0.f, 0.f, 0.f, 0.f};
#pragma unroll
                for (int ks = 0; ks < 5; ++ks) acc[tl] = __builtin_amdgcn_mfma_f32_16x16x32_bf16(wf[ks * 4 + tl], __builtin_bit_cast(bf8, hb[ks]), acc[tl], 0, 0, 0);
                if (col < 2) *(LAS f4*)(lds + L_RED + w * 512 + col * 256 + tl * 64 + q4 * 16) = acc[tl];
            }
            bar();
            if (ct < 32) {
                const LAS f2* q = (const LAS f2*)(lds + L_RED + (ct >> 4) * 256 + (ct & 15) * 8);
                const f2 g = (q[0] + q[64]) + (q[128] + q[192]), u = (q[16] + q[80]) + (q[144] + q[208]);
                const float a0 = g.x * __builtin_amdgcn_rcpf(1.f + __expf(-g.x)) * u.x, a1 = g.y * __builtin_amdgcn_rcpf(1.f + __expf(-g.y)) * u.y;
                __builtin_amdgcn_raw_buffer_store_b32(san(pk(a0, a1)), wr, W_A + ab * 128 + ct * 4, 0, SC1);
            }
            STAMP(11);
        } else if (isP4) {
            bf8 wf[14];
            const int ks0 = 14 * w - (w > 2 ? 1 : 0);
#pragma unroll
            for (int i = 0; i < 14; ++i) {
                const int ks = ks0 + i < 54 ? ks0 + i : 53;
                const u16* src = p.wd + (32 * ks + (lane >> 1)) * D_ + 16 * (ab - 54) + 8 * (lane & 1);
                __builtin_amdgcn_global_load_lds((const GAS void*)src, (LAS void*)(lds + w * 14336 + i * 1024), 16, 0, 0);
            }
            DRAIN();
            const LAS char* tb = lds + w * 14336 + (8 * q4 + (col >> 2)) * 32 + (col & 3) * 8;
#pragma unroll
            for (int i = 0; i < 14; ++i) {
                const s4 lo = __builtin_amdgcn_ds_read_tr16_b64_v4i16((LAS s4*)(tb + i * 1024));
                const s4 hi = __builtin_amdgcn_ds_read_tr16_b64_v4i16((LAS s4*)(tb + i * 1024 + 128));
                const v2u l2 = __builtin_bit_cast(v2u, lo), h2 = __builtin_bit_cast(v2u, hi);
                const v4u f = {l2.x, l2.y, h2.x, h2.y};
                wf[i] = __builtin_bit_cast(bf8, f);
            }
            waitP(); top2();
            WINNERS();
            const u64 tw = now() + DLY_D;
            const int d = 16 * (ab - 54) + 2 * (ct & 7);
            u32 hw = 0, xw = 0, gw = 0;
            if (w == 0) {
                while (!__all(__hip_atomic_load(wsq + W_FLAGM / 8 + (lane < 60 ? lane : 0), RLX) == epoch) && now() < tmo) {}
                const u32 off = ((ct >> 3) & 1 ? row2 : row1) + d * 2;
                for (;;) {
                    CFENCE();
                    hw = __builtin_amdgcn_raw_buffer_load_b32(wr, off, 0, SC1);
                    xw = __builtin_amdgcn_raw_buffer_load_b32(wr, off + 1280, 0, SC1);
                    gw = __builtin_amdgcn_raw_buffer_load_b32(wr, W_MOD + (5 * D_ + d) * 2, 0, SC1);
                    if (__all(hw != POISON && xw != POISON) || now() > tmo) break;
                }
            }
            wait_until(tw);
            STAMP(10);
            const int nks = w < 2 ? 14 : 13;
            v4u av[14];
            for (;;) {
                CFENCE();
                bool ok = true;
#pragma unroll
                for (int i = 0; i < 14; ++i) {
                    av[i] = z4;
                    if (col < 2 && i < nks) av[i] = __builtin_amdgcn_raw_buffer_load_b128(wr, W_A + (ks0 + i) * 128 + col * 64 + q4 * 16, 0, SC1);
                }
#pragma unroll
                for (int i = 0; i < 14; ++i) ok &= clean4(av[i]);
                if (__all(ok) || now() > tmo) break;
            }
            STAMP(11);
            f4 a0 = {0.f, 0.f, 0.f, 0.f}, a1 = {0.f, 0.f, 0.f, 0.f};
#pragma unroll
            for (int i = 0; i < 14; i += 2) {
                a0 = __builtin_amdgcn_mfma_f32_16x16x32_bf16(wf[i], __builtin_bit_cast(bf8, av[i]), a0, 0, 0, 0);
                a1 = __builtin_amdgcn_mfma_f32_16x16x32_bf16(wf[i + 1], __builtin_bit_cast(bf8, av[i + 1]), a1, 0, 0, 0);
            }
            if (col < 2) *(LAS f4*)(lds + L_RED + w * 128 + col * 64 + q4 * 16) = a0 + a1;
            bar();
            if (ct < 16) {
                const LAS f2* q = (const LAS f2*)(lds + L_RED + (ct >> 3) * 64 + (ct & 7) * 8);
                const f2 y = (q[0] + q[16]) + (q[32] + q[48]);
                const u32 key = (ct >> 3) ? g2 : g1;
                u32 u = (key & ~0x7FFu) | 0x400u;
                u = (u & 0x80000000u) ? (u & 0x7FFFFFFFu) : ~u;
                const float sg = __builtin_amdgcn_rcpf(1.f + __expf(-__uint_as_float(u)));
                const f2 o = bfup(gw) * (bfup(hw) + y * sg) + bfup(xw);
                *(u32*)(p.out + ((ct >> 3) ? t2 : t1) * D_ + d) = pk2(o);
            }
            STAMP(12);
        }
        else { waitP(); top2(); }
    }
}

extern "C" void kernel_launch(void* const* d_in, const int* in_sizes, int n_in,
                              void* d_out, int out_size, void* d_ws, size_t ws_size,
                              hipStream_t stream) {
    Params p;
    p.x = (const u16*)d_in[0]; p.k = (const u16*)d_in[1]; p.v = (const u16*)d_in[2];
    p.sh = (const u16*)d_in[3]; p.sk = (const u16*)d_in[4]; p.sD = (const u16*)d_in[5];
    p.wr = (const u16*)d_in[7]; p.wg = (const u16*)d_in[8]; p.wu = (const u16*)d_in[9]; p.wd = (const u16*)d_in[10];
    p.c = (const u16*)d_in[11]; p.wm = (const u16*)d_in[12];
    p.out = (u16*)d_out; p.ws = (char*)d_ws;
    fused_kernel<<<256, 576, 0, stream>>>(p);
}
```

```cpp
#include <hip/hip_runtime.h>
#include <stdint.h>

typedef unsigned short u16;
typedef unsigned u32;
typedef unsigned long long u64;
typedef u32 v2u __attribute__((ext_vector_type(2)));
typedef u32 v4u __attribute__((ext_vector_type(4)));
typedef float f2 __attribute__((ext_vector_type(2)));
typedef float f4 __attribute__((ext_vector_type(4)));
typedef __bf16 bf2 __attribute__((ext_vector_type(2)));
typedef __bf16 bf8 __attribute__((ext_vector_type(8)));
typedef short s4 __attribute__((ext_vector_type(4)));
#define LAS __attribute__((address_space(3)))
#define GAS __attribute__((address_space(1)))
#define RLX __ATOMIC_RELAXED, __HIP_MEMORY_SCOPE_AGENT
#define SC1 16
#define POISON 0xFFFFFFFFu
#define OUT_AUX 16

constexpr int S_ = 2048, D_ = 640, F_ = 1728;
constexpr u32 W_EPOCH = 0;
constexpr u32 W_FLAGK = 4096;
constexpr u32 W_HINT = 12288;
constexpr u32 W_MOD = 24576;
constexpr u32 W_R = 32768;
constexpr u32 RS = 2048 + 256;
constexpr u32 W_A = 49152;
constexpr u32 W_FLAGM = 57344;
constexpr u32 W_FLAGP = 65536;
constexpr u32 W_ROWS = 131072;
constexpr u32 W_KVT = 2u << 20;
constexpr u32 YS = 10240;
constexpr u32 W_Y = 5u << 20;
constexpr u32 W_END = 8u << 20;
constexpr u32 W_STAMP = 16u << 20;
constexpr u32 CHS = 21312, KCS = 4160, L_R = 16640;
constexpr u32 L_CTL = 4 * CHS;
constexpr u32 L_LN1 = L_CTL + 64;
constexpr u32 L_CC = L_LN1 + 512;
constexpr u32 L_P2 = L_CC + 64;
constexpr u32 L_T2 = L_P2 + 512;
constexpr u32 L_RED = L_T2 + 64;
constexpr u32 L_MODP = L_RED + 2048;
constexpr u32 L_END = L_MODP + 1024;

#define DLY_A 70
#define DLY_T 0
#define DLY_H 150
#define DLY_W 10
#define DLY_S 5
#define DLY_P 10
#define SK_POS 2
#define DLY_M 130
#define DLY_M2 130
#define DLY_Y 60
#define DLY_R 40
#define DLY_D 110

struct Params {
    const u16 *x, *k, *v, *sh, *sk, *sD, *wr, *wg, *wu, *wd, *c, *wm;
    u16* out;
    char* ws;
};

__device__ __forceinline__ float bflo(u32 w) { return __uint_as_float(w << 16); }
__device__ __forceinline__ float bfhi(u32 w) { return __uint_as_float(w & 0xFFFF0000u); }
__device__ __forceinline__ f2 bfup(u32 w) { f2 r = {bflo(w), bfhi(w)}; return r; }
__device__ __forceinline__ u32 pk(float lo, float hi) { f2 a = {lo, hi}; return __builtin_bit_cast(u32, __builtin_convertvector(a, bf2)); }
__device__ __forceinline__ u32 pk2(f2 a) { return __builtin_bit_cast(u32, __builtin_convertvector(a, bf2)); }
__device__ __forceinline__ f2 rnd(f2 a) { return bfup(pk2(a)); }
__device__ __forceinline__ float rnd1(float a) { return bflo(pk(a, a)); }
__device__ __forceinline__ u32 san(u32 w) { return w < 0xFFFFFFFEu ? w : 0xFFFFFFFEu; }
__device__ __forceinline__ u32 umax(u32 a, u32 b) { return a > b ? a : b; }
__device__ __forceinline__ u32 umin(u32 a, u32 b) { return a < b ? a : b; }
__device__ __forceinline__ bool clean4(v4u d) { return umax(umax(d.x, d.y), umax(d.z, d.w)) != POISON; }
__device__ __forceinline__ u64 now() { return __builtin_amdgcn_s_memrealtime(); }
template <int C> __device__ __forceinline__ u32 dppu(u32 x) { return (u32)__builtin_amdgcn_mov_dpp((int)x, C, 0xf, 0xf, true); }
template <int C> __device__ __forceinline__ float dppf(float x) { return __uint_as_float(dppu<C>(__float_as_uint(x))); }
constexpr int DPP_X1 = 0xB1, DPP_X2 = 0x4E, DPP_HM = 0x141, DPP_M = 0x140, DPP_ROR4 = 0x124, DPP_ROR8 = 0x128;
__device__ __forceinline__ float add32(float a, float b) {
    auto s = __builtin_amdgcn_permlane32_swap(__float_as_uint(a), __float_as_uint(b), false, false);
    return __uint_as_float(s[0]) + __uint_as_float(s[1]);
}
__device__ __forceinline__ float add16(float a, float b) {
    auto s = __builtin_amdgcn_permlane16_swap(__float_as_uint(a), __float_as_uint(b), false, false);
    return __uint_as_float(s[0]) + __uint_as_float(s[1]);
}
__device__ __forceinline__ void top2_merge(u32& a1, u32& a2, u32 b1, u32 b2) {
    u32 t = umin(a1, b1), u = umax(a2, b2);
    a1 = umax(a1, b1); a2 = umax(t, u);
}
__device__ __forceinline__ void wait_until(u64 t) { while (now() < t) __builtin_amdgcn_s_sleep(1); }
__device__ __forceinline__ void bar() { asm volatile("s_waitcnt lgkmcnt(0)\n\ts_barrier" ::: "memory"); }
#define DRAIN() asm volatile("s_waitcnt vmcnt(0)" ::: "memory")
#define CFENCE() asm volatile("" ::: "memory")
#define TMO 300000ull

#define STAMP(i) do {} while (0)

__global__ void __launch_bounds__(576) fused_kernel(Params p) {
    __shared__ __attribute__((aligned(16))) char smem[L_END];
    LAS char* lds = (LAS char*)smem;
    const int tid = threadIdx.x, b = blockIdx.x, lane = tid & 63;
    const int wave = __builtin_amdgcn_readfirstlane(tid >> 6);
    const u64 tstart = now(), tmo = tstart + TMO;
    const __amdgpu_buffer_rsrc_t wr = __builtin_amdgcn_make_buffer_rsrc(p.ws, 0, W_END, 0x00020000);
    GAS u64* const wsq = (GAS u64*)p.ws;
    LAS u32* const ctl = (LAS u32*)(lds + L_CTL);
    const u64 e0 = *(const __attribute__((address_space(4))) u64*)(uintptr_t)p.ws;
    const u64 epoch = e0 * 0xD1342543DE82EF95ull + 0x9E3779B97F4A7C15ull;
    const int tt = 16 * (b & 7) + ((b >> 3) & 15), hf = b >> 7;
    const int ab = b - 160;

    if (wave < 5) {
        const int t0 = 16 * tt + 8 * (lane >> 5), ch = 320 * hf + 64 * wave + 2 * (lane & 31);
        u32 kw[11], vw[8];
#pragma unroll
        for (int i = 0; i < 11; ++i) { int t = t0 - 3 + i; kw[i] = *(const u32*)(p.k + (t < 0 ? 0 : t) * D_ + ch); }
#pragma unroll
        for (int i = 0; i < 8; ++i) vw[i] = *(const u32*)(p.v + (t0 + i) * D_ + ch);
        const v4u sw = *(const v4u*)(p.sh + ch * 4);
        {
            if (t0 == 0) { kw[0] = 0; kw[1] = 0; kw[2] = 0; }
            const f2 s0 = {bflo(sw.x), bflo(sw.z)}, s1 = {bfhi(sw.x), bfhi(sw.z)}, s2 = {bflo(sw.y), bflo(sw.w)}, s3 = {bfhi(sw.y), bfhi(sw.w)};
            f2 kf[11], u[8];
#pragma unroll
            for (int i = 0; i < 11; ++i) kf[i] = bfup(kw[i]);
#pragma unroll
            for (int i = 0; i < 8; ++i) u[i] = (kf[i] * s3 + kf[i + 1] * s2 + kf[i + 2] * s1 + kf[i + 3] * s0) * bfup(vw[i]);
            v4u c0 = {san(pk(u[7].x, u[6].x)), san(pk(u[5].x, u[4].x)), san(pk(u[3].x, u[2].x)), san(pk(u[1].x, u[0].x))};
            v4u c1 = {san(pk(u[7].y, u[6].y)), san(pk(u[5].y, u[4].y)), san(pk(u[3].y, u[2].y)), san(pk(u[1].y, u[0].y))};
#pragma unroll
            for (int i = 0; i < 4; ++i) { auto t = __builtin_amdgcn_permlane16_swap(c0[i], c1[i], false, false); c0[i] = t[0]; c1[i] = t[1]; }
            const u32 off = W_KVT + (80 * hf + 16 * wave + ((lane & 15) >> 1)) * 16384 + (255 - (t0 >> 3)) * 64 + (2 * (lane & 1) + ((lane >> 4) & 1)) * 16;
            asm volatile("" : "+v"(c0), "+v"(c1) : "s"(epoch), "v"(off));
            STAMP(1);
            bar();
            __builtin_amdgcn_raw_buffer_store_b128(c0, wr, off, 0, SC1);
            __builtin_amdgcn_raw_buffer_store_b128(c1, wr, off + 8 * 16384, 0, SC1);
        }
        STAMP(2);
        auto poison2 = [&]() __attribute__((always_inline)) {
            const v4u ps = {POISON, POISON, POISON, POISON};
            __builtin_amdgcn_raw_buffer_store_b128(ps, wr, W_Y + b * YS + tid * 16, 0, SC1);
            __builtin_amdgcn_raw_buffer_store_b128(ps, wr, W_Y + b * YS + 5120 + tid * 16, 0, SC1);
            __builtin_amdgcn_raw_buffer_store_b128(ps, wr, W_ROWS + b * 5120 + tid * 16, 0, SC1);
            if (tid < 4) { const v2u p2 = {POISON, POISON}; __builtin_amdgcn_raw_buffer_store_b64(p2, wr, W_R + tid * RS + b * 8, 0, SC1); }
            if (b >= 160 && b < 214 && tid >= 64 && tid < 72) __builtin_amdgcn_raw_buffer_store_b128(ps, wr, W_A + (b - 160) * 128 + (tid - 64) * 16, 0, SC1);
        };
        if (b < 160) {
            bar();
            if (b < 96 && tid == 0) __hip_atomic_store(wsq + W_HINT / 8 + b * 16, epoch, RLX);
        } else if (DLY_T) wait_until(now() + DLY_T);
        const u64 tM = now() + DLY_M;
        STAMP(11);
        const int tp = lane & 3, cb = lane >> 2;
        const int tokE = 8 * b + 2 * tp;
        v2u xw[2][2];
#pragma unroll
        for (int i = 0; i < 2; ++i)
#pragma unroll
            for (int o = 0; o < 2; ++o) xw[o][i] = *(const v2u*)(p.x + (tokE + o) * D_ + 64 * (2 * wave + i) + 4 * cb);
        v2u wrw[2];
#pragma unroll
        for (int i = 0; i < 2; ++i) wrw[i] = *(const v2u*)(p.wr + 64 * (2 * wave + i) + 4 * cb);
        poison2();
        DRAIN();
        {
            u32 old = 0;
            if (lane == 0) old = __hip_atomic_fetch_add(ctl + 1, 1u, __ATOMIC_RELAXED, __HIP_MEMORY_SCOPE_WORKGROUP);
            if (__builtin_amdgcn_readfirstlane(old) == 4 && lane < 8) __hip_atomic_store(wsq + W_FLAGP / 8 + lane * 256 + b, epoch, RLX);
        }
        STAMP(3);
        f2 xf[2][4];
#pragma unroll
        for (int o = 0; o < 2; ++o) { xf[o][0] = bfup(xw[o][0].x); xf[o][1] = bfup(xw[o][0].y); xf[o][2] = bfup(xw[o][1].x); xf[o][3] = bfup(xw[o][1].y); }
        const int tl = 2 * (lane & 3) + ((lane >> 2) & 1);
        float mE, mO, rE, rO;
        {
            float s1[2], s2[2];
#pragma unroll
            for (int o = 0; o < 2; ++o) {
                f2 a = (xf[o][0] + xf[o][1]) + (xf[o][2] + xf[o][3]);
                f2 q = xf[o][0] * xf[o][0] + xf[o][1] * xf[o][1] + xf[o][2] * xf[o][2] + xf[o][3] * xf[o][3];
                s1[o] = a.x + a.y; s2[o] = q.x + q.y;
            }
            float q12 = add16(add32(s1[0], s1[1]), add32(s2[0], s2[1]));
            q12 += dppf<DPP_ROR8>(q12); q12 += dppf<DPP_ROR4>(q12);
            if ((lane & 12) == 0) *(LAS float*)(lds + L_LN1 + (2 * tp + (lane >> 5)) * 64 + (((lane >> 4) & 1) * 8 + wave) * 4) = q12;
            if (wave == 0) {
                wait_until(b < 160 ? tM : tM + DLY_M2);
                while (!__all(__hip_atomic_load(wsq + W_FLAGM / 8 + (b & 7) * 64 + (lane < 60 ? lane : 0), RLX) == epoch) && now() < tmo) __builtin_amdgcn_s_sleep(8);
            }
            asm volatile("s_waitcnt lgkmcnt(0)" ::: "memory");
            if (lane == 0) __hip_atomic_fetch_add(ctl + 2, 1u, __ATOMIC_RELAXED, __HIP_MEMORY_SCOPE_WORKGROUP);
            while (__hip_atomic_load(ctl + 2, __ATOMIC_RELAXED, __HIP_MEMORY_SCOPE_WORKGROUP) < 5u) __builtin_amdgcn_s_sleep(1);
            CFENCE();
            const LAS float* q = (const LAS float*)(lds + L_LN1 + tl * 64);
            float a = ((q[0] + q[1]) + (q[2] + q[3])) + q[4], c = ((q[8] + q[9]) + (q[10] + q[11])) + q[12];
            float m = a * (1.f / D_);
            const float rs = __builtin_amdgcn_rcpf(rnd1(__builtin_amdgcn_sqrtf(rnd1(rnd1(fmaxf(c * (1.f / D_) - m * m, 0.f)) + 1e-5f))));
            m = rnd1(m);
            float m2 = dppf<DPP_ROR4>(m), rs2 = dppf<DPP_ROR4>(rs);
            const bool od = (lane >> 2) & 1;
            mE = od ? m2 : m; mO = od ? m : m2; rE = od ? rs2 : rs; rO = od ? rs : rs2;
        }
        STAMP(4);
        f2 gh[2][4], cw[4], c1b[4], sb[4], gb[4];
        float C1, C2;
        {
            v2u mw[6][2];
#pragma unroll
            for (int j = 0; j < 6; ++j)
#pragma unroll
                for (int i = 0; i < 2; ++i) mw[j][i] = __builtin_amdgcn_raw_buffer_load_b64(wr, W_MOD + (j * D_ + 64 * (2 * wave + i) + 4 * cb) * 2, 0, SC1);
            f2 c1 = {0.f, 0.f}, c2 = {0.f, 0.f};
#pragma unroll
            for (int j = 0; j < 4; ++j) {
                const int i = j >> 1;
#define PICK(a) ((j & 1) ? (a)[i].y : (a)[i].x)
                const f2 sa = bfup(PICK(mw[0])), ca = rnd(bfup(PICK(mw[1])) + 1.f), ga = bfup(PICK(mw[2])), w = bfup(PICK(wrw));
                sb[j] = bfup(PICK(mw[3])); c1b[j] = rnd(bfup(PICK(mw[4])) + 1.f); gb[j] = bfup(PICK(mw[5]));
#undef PICK
                gh[0][j] = ga * rnd(rnd(rnd(rnd(xf[0][j] - mE) * rE) * ca) + sa);
                gh[1][j] = ga * rnd(rnd(rnd(rnd(xf[1][j] - mO) * rO) * ca) + sa);
                cw[j] = c1b[j] * w;
                c1 += cw[j]; c2 += sb[j] * w;
            }
            float q = add32(c1.x + c1.y, c2.x + c2.y);
            q = add16(q, q);
            q += dppf<DPP_ROR8>(q); q += dppf<DPP_ROR4>(q);
            if ((lane & 31) == 0) *(LAS float*)(lds + L_CC + (lane >> 5) * 32 + wave * 4) = q;
            asm volatile("s_waitcnt lgkmcnt(0)" ::: "memory");
        }
        STAMP(5);
        if (wave == 0) {
            for (;;) {
                bool ok = true;
#pragma unroll
                for (int j = 0; j < 4; ++j) ok &= __hip_atomic_load(wsq + W_FLAGP / 8 + (b & 7) * 256 + 64 * j + lane, RLX) == epoch;
                if (__all(ok) || now() > tmo) break;
                __builtin_amdgcn_s_sleep(8);
            }
            if (b == 0 && lane == 0) __hip_atomic_store(wsq + W_EPOCH / 8, epoch, RLX);
        } else if (wave == 1 && b >= 160) {
            while (__hip_atomic_load(wsq + W_HINT / 8 + ab * 16, RLX) != epoch && now() < tmo) __builtin_amdgcn_s_sleep(4);
            wait_until(now() + DLY_H);
        }
        STAMP(12);
        bar();
        {
            const LAS float* q = (const LAS float*)(lds + L_CC);
            C1 = ((q[0] + q[1]) + (q[2] + q[3])) + q[4]; C2 = ((q[8] + q[9]) + (q[10] + q[11])) + q[12];
        }
        wait_until(now() + DLY_Y);
        STAMP(6);
        v4u yw[2];
        for (;;) {
            CFENCE();
            yw[0] = __builtin_amdgcn_raw_buffer_load_b128(wr, W_Y + b * YS + wave * 2048 + lane * 16, 0, SC1);
            yw[1] = __builtin_amdgcn_raw_buffer_load_b128(wr, W_Y + b * YS + wave * 2048 + 1024 + lane * 16, 0, SC1);
            if (__all(clean4(yw[0]) && clean4(yw[1])) || now() > tmo) break;
        }
        STAMP(7);
        f2 x1[2][4];
        x1[0][0] = gh[0][0] * bfup(yw[0].x) + xf[0][0]; x1[0][1] = gh[0][1] * bfup(yw[0].y) + xf[0][1];
        x1[1][0] = gh[1][0] * bfup(yw[0].z) + xf[1][0]; x1[1][1] = gh[1][1] * bfup(yw[0].w) + xf[1][1];
        x1[0][2] = gh[0][2] * bfup(yw[1].x) + xf[0][2]; x1[0][3] = gh[0][3] * bfup(yw[1].y) + xf[0][3];
        x1[1][2] = gh[1][2] * bfup(yw[1].z) + xf[1][2]; x1[1][3] = gh[1][3] * bfup(yw[1].w) + xf[1][3];
        {
            float s1[2], s2[2], s3[2];
#pragma unroll
            for (int o = 0; o < 2; ++o) {
                f2 a = (x1[o][0] + x1[o][1]) + (x1[o][2] + x1[o][3]);
                f2 q = x1[o][0] * x1[o][0] + x1[o][1] * x1[o][1] + x1[o][2] * x1[o][2] + x1[o][3] * x1[o][3];
                f2 r = x1[o][0] * cw[0] + x1[o][1] * cw[1] + x1[o][2] * cw[2] + x1[o][3] * cw[3];
                s1[o] = a.x + a.y; s2[o] = q.x + q.y; s3[o] = r.x + r.y;
            }
            float q12 = add16(add32(s1[0], s1[1]), add32(s2[0], s2[1]));
            float q3 = add32(s3[0], s3[1]); q3 = add16(q3, q3);
            q12 += dppf<DPP_ROR8>(q12); q3 += dppf<DPP_ROR8>(q3);
            q12 += dppf<DPP_ROR4>(q12); q3 += dppf<DPP_ROR4>(q3);
            if ((lane & 12) == 0) {
                LAS float* q = (LAS float*)(lds + L_P2 + (2 * tp + (lane >> 5)) * 64);
                q[((lane >> 4) & 1) * 5 + wave] = q12;
                q[10 + wave] = q3;
            }
        }
        bar();
        u32 keyE, keyO, k1, k2;
        {
            const LAS f4* q = (const LAS f4*)(lds + L_P2 + tl * 64);
            const f4 a = q[0], c = q[1], d = q[2], e = q[3];
            const float S1 = ((a.x + a.y) + (a.z + a.w)) + c.x, S2 = ((c.y + c.z) + (c.w + d.x)) + d.y, S3 = ((d.z + d.w) + (e.x + e.y)) + e.z;
            float m = S1 * (1.f / D_);
            const float rs = __builtin_amdgcn_rcpf(rnd1(__builtin_amdgcn_sqrtf(rnd1(rnd1(fmaxf(S2 * (1.f / D_) - m * m, 0.f)) + 1e-5f))));
            m = rnd1(m);
            const float r = rs * (S3 - m * C1) + C2;
            u32 u = __float_as_uint(r);
            u = (u & 0x80000000u) ? ~u : (u | 0x80000000u);
            const u32 key = san((u & ~0x7FFu) | (u32)(2047 - (8 * b + tl)));
            const u32 key2 = dppu<DPP_ROR4>(key);
            k1 = umax(key, key2); k2 = umin(key, key2);
            top2_merge(k1, k2, dppu<DPP_X1>(k1), dppu<DPP_X1>(k2));
            top2_merge(k1, k2, dppu<DPP_X2>(k1), dppu<DPP_X2>(k2));
            if (tid < 4) { const v2u kk = {k1, k2}; __builtin_amdgcn_raw_buffer_store_b64(kk, wr, W_R + tid * RS + b * 8, 0, SC1); }
            STAMP(8);
            const float m2 = dppf<DPP_ROR4>(m), rs2 = dppf<DPP_ROR4>(rs);
            const bool od = (lane >> 2) & 1;
            mE = od ? m2 : m; mO = od ? m : m2; rE = od ? rs2 : rs; rO = od ? rs : rs2;
            keyE = od ? key2 : key; keyO = od ? key : key2;
        }
        u32 ow[2][4];
#pragma unroll
        for (int o = 0; o < 2; ++o) {
            const float m = o ? mO : mE, rs = o ? rO : rE;
            const u32 key = o ? keyO : keyE;
            u32 hw[4];
#pragma unroll
            for (int j = 0; j < 4; ++j) {
                hw[j] = san(pk2(rnd(rnd(rnd(x1[o][j] - m) * rs) * c1b[j]) + sb[j]));
                ow[o][j] = pk2(gb[j] * bfup(hw[j]) + x1[o][j]);
            }
            if (key == k1 || key == k2) {
                const u32 off = W_ROWS + b * 5120 + (key == k1 ? 0 : 2560) + (128 * wave + 4 * cb) * 2;
                v2u a = {hw[0], hw[1]}, c = {hw[2], hw[3]};
                __builtin_amdgcn_raw_buffer_store_b64(a, wr, off, 0, SC1);
                __builtin_amdgcn_raw_buffer_store_b64(c, wr, off + 128, 0, SC1);
            }
        }
        STAMP(9);
        {
            const __amdgpu_buffer_rsrc_t orr = __builtin_amdgcn_make_buffer_rsrc(p.out, 0, S_ * D_ * 2, 0x00020000);
#pragma unroll
            for (int o = 0; o < 2; ++o) {
                const u32 off = ((tokE + o) * D_ + 128 * wave + 4 * cb) * 2;
                v2u a0 = {ow[o][0], ow[o][1]}, a1 = {ow[o][2], ow[o][3]};
                __builtin_amdgcn_raw_buffer_store_b64(a0, orr, off, 0, SC1);
                __builtin_amdgcn_raw_buffer_store_b64(a1, orr, off + 128, 0, SC1);
            }
            DRAIN();
#pragma unroll
            for (int o = 0; o < 2; ++o) {
                const u32 key = o ? keyO : keyE;
                if (key == k1 || key == k2) {
                    const u32 off = W_ROWS + b * 5120 + (key == k1 ? 0 : 2560) + 1280 + (128 * wave + 4 * cb) * 2;
                    v2u a = {san(pk2(x1[o][0])), san(pk2(x1[o][1]))}, c = {san(pk2(x1[o][2])), san(pk2(x1[o][3]))};
                    __builtin_amdgcn_raw_buffer_store_b64(a, wr, off, 0, SC1);
                    __builtin_amdgcn_raw_buffer_store_b64(c, wr, off + 128, 0, SC1);
                }
            }
        }
        STAMP(10);
        return;
    }

    const int w = wave - 5, ct = tid - 320;
    const int col = lane & 15, q4 = lane >> 4;
    const bool isP3 = ab >= 0 && ab < 54, isP4 = ab >= 54 && ab < 94;
    u32 g1, g2;
    {
        const v4u ps = {POISON, POISON, POISON, POISON};
        const u32 off = W_KVT + (80 * hf + 20 * w + (lane >> 3)) * 16384 + (254 - 2 * tt) * 64 + (lane & 7) * 16;
        asm volatile("" :: "s"(epoch), "v"(off));
        if (DLY_P) wait_until(now() + DLY_P);
        __builtin_amdgcn_raw_buffer_store_b128(ps, wr, off, 0, SC1);
        __builtin_amdgcn_raw_buffer_store_b128(ps, wr, off + 8 * 16384, 0, SC1);
        if (lane < 32) __builtin_amdgcn_raw_buffer_store_b128(ps, wr, off + 16 * 16384, 0, SC1);
        if (ct < 16) ctl[ct] = 0;
    }
    auto flagK = [&]() __attribute__((always_inline)) {
        DRAIN();
        if (lane == 0) __hip_atomic_store(wsq + W_FLAGK / 8 + (hf * 4 + w) * 128 + tt, epoch, RLX);
    };
    auto top2 = [&]() __attribute__((always_inline)) {
        bar();
        bar();
        wait_until(now() + DLY_R);
        STAMP(7);
        v4u a, c;
        for (;;) {
            CFENCE();
            a = __builtin_amdgcn_raw_buffer_load_b128(wr, W_R + w * RS + lane * 16, 0, SC1);
            c = __builtin_amdgcn_raw_buffer_load_b128(wr, W_R + w * RS + 1024 + lane * 16, 0, SC1);
            if (__all(clean4(a) && clean4(c)) || now() > tmo) break;
        }
        STAMP(8);
        g1 = a.x; g2 = a.y;
        top2_merge(g1, g2, a.z, a.w);
        u32 h1 = c.x, h2 = c.y;
        top2_merge(h1, h2, c.z, c.w);
        top2_merge(g1, g2, h1, h2);
        top2_merge(g1, g2, dppu<DPP_X1>(g1), dppu<DPP_X1>(g2));
        top2_merge(g1, g2, dppu<DPP_X2>(g1), dppu<DPP_X2>(g2));
        top2_merge(g1, g2, dppu<DPP_HM>(g1), dppu<DPP_HM>(g2));
        top2_merge(g1, g2, dppu<DPP_M>(g1), dppu<DPP_M>(g2));
        { auto s = __builtin_amdgcn_permlane16_swap(g1, g1, false, false); auto t = __builtin_amdgcn_permlane16_swap(g2, g2, false, false);
          g1 = s[0]; g2 = t[0]; top2_merge(g1, g2, s[1], t[1]); }
        { auto s = __builtin_amdgcn_permlane32_swap(g1, g1, false, false); auto t = __builtin_amdgcn_permlane32_swap(g2, g2, false, false);
          g1 = s[0]; g2 = t[0]; top2_merge(g1, g2, s[1], t[1]); }
        STAMP(9);
    };
#define WINNERS() const int t1 = 2047 - (g1 & 0x7FF), t2 = 2047 - (g2 & 0x7FF); \
    const u32 row1 = W_ROWS + (t1 >> 3) * 5120, row2 = W_ROWS + (t2 >> 3) * 5120 + ((t1 >> 3) == (t2 >> 3) ? 2560 : 0); \
    const v4u z4 = {0, 0, 0, 0};
    if (b < 160) {
        const int ch = 4 * b + w;
        LAS char* cbase = lds + w * CHS;
        v4u c1[4];
        u32 db;
        auto ldk = [&]() __attribute__((always_inline)) {
#pragma unroll
            for (int j = 0; j < 4; ++j) c1[j] = *(const v4u*)(p.sk + ch * S_ + 8 * (64 * j + lane));
            db = p.sD[ch];
        };
        if (SK_POS == 0) ldk();
        flagK();
        if (SK_POS == 1) ldk();
        STAMP(1);
        bar();
        const u64 tk = now() + DLY_A;
        if (SK_POS == 2) { if (DLY_S) wait_until(now() + DLY_S); ldk(); }
        const int fk = ((b / 80) * 4 + (b % 80) / 20) * 128 + 96 - 32 * w + (lane & 31);
        u64 fl = __hip_atomic_load(wsq + W_FLAGK / 8 + fk, RLX);
        LAS char* rbase = cbase + L_R + ((96 * w) & 127);
        {
            const v4u z4 = {0, 0, 0, 0};
            if (lane < 30) *(LAS v4u*)(rbase + 4096 + lane * 16) = z4;
            if (lane == 0) c1[0].x = (c1[0].x & 0xFFFF0000u) | (pk(bflo(c1[0].x) + bflo(db), 0.f) & 0xFFFFu);
            const u32 k0 = __builtin_amdgcn_readfirstlane(c1[0].x), k1 = __builtin_amdgcn_readfirstlane(c1[0].y);
            if (lane < 8) {
                v4u h = z4;
                if (lane == 4) h.w = k0 << 16;
                if (lane == 5) h.w = k0;
                if (lane == 6) { h.z = k0 << 16; h.w = (k0 >> 16) | (k1 << 16); }
                if (lane == 7) { h.z = k0; h.w = k1; }
                *(LAS v4u*)(cbase + (lane & 3) * KCS + (lane >> 2) * 16) = h;
            }
#pragma unroll
            for (int j = 0; j < 4; ++j) {
                const u32 n0 = j < 3 ? __builtin_amdgcn_readfirstlane(c1[j < 3 ? j + 1 : j].x) : 0u, n1 = j < 3 ? __builtin_amdgcn_readfirstlane(c1[j < 3 ? j + 1 : j].y) : 0u;
                const u32 e[6] = {c1[j].x, c1[j].y, c1[j].z, c1[j].w,
                                  (u32)__builtin_amdgcn_update_dpp((int)n0, (int)c1[j].x, 0x130, 0xf, 0xf, false), (u32)__builtin_amdgcn_update_dpp((int)n1, (int)c1[j].y, 0x130, 0xf, 0xf, false)};
                u32 o[5];
#pragma unroll
                for (int i = 0; i < 5; ++i) o[i] = __builtin_amdgcn_alignbit(e[i + 1], e[i], 16);
                LAS char* dst = cbase + (64 * j + lane + 2) * 16;
#pragma unroll
                for (int m = 0; m < 2; ++m) {
                    const v4u a = {o[m], o[m + 1], o[m + 2], o[m + 3]}, c = {e[m + 1], e[m + 2], e[m + 3], e[m + 4]};
                    *(LAS v4u*)(dst + (2 * m) * KCS) = a;
                    *(LAS v4u*)(dst + (2 * m + 1) * KCS) = c;
                }
            }
        }
        const LAS char* pa = cbase + (col & 3) * KCS + (2 * q4 + (col >> 2)) * 8;
        const LAS char* pb = rbase + 480 + 16 * q4 - 32 * col;
        const LAS char* pa2 = pa + 8;
        asm volatile("" : "+v"(pa2));
        auto lda = [&](int o) __attribute__((always_inline)) {
            const v2u lo = *(const LAS v2u*)(pa + o), hi = *(const LAS v2u*)(pa2 + o);
            const v4u f = {lo.x, lo.y, hi.x, hi.y};
            return __builtin_bit_cast(bf8, f);
        };
#define LDA(a, r) lda((a) * 512 + (r) * 64)
#define LDB(bb, r) (*(const LAS bf8*)(pb + 3584 - (bb) * 512 + (r) * 64))
        bf8 A[10], B[10];
#pragma unroll
        for (int j = 0; j < 8; ++j) A[j] = LDA(j, 0);
        while (!__all(fl == epoch) && now() < tmo) { __builtin_amdgcn_s_sleep(2); fl = __hip_atomic_load(wsq + W_FLAGK / 8 + fk, RLX); }
        STAMP(2);
        wait_until(tk);
        STAMP(3);
        {
            v4u r[4];
            for (;;) {
                CFENCE();
#pragma unroll
                for (int j = 0; j < 4; ++j) r[j] = __builtin_amdgcn_raw_buffer_load_b128(wr, W_KVT + b * 16384 + w * 4096 + j * 1024 + lane * 16, 0, SC1);
                if (__all(clean4(r[0]) && clean4(r[1]) && clean4(r[2]) && clean4(r[3])) || now() > tmo) break;
            }
            STAMP(10);
            LAS char* dst = lds + (lane & 3) * CHS + L_R + ((96 * lane) & 127) + (64 * w + (lane >> 2)) * 16;
#pragma unroll
            for (int j = 0; j < 4; ++j) *(LAS v4u*)(dst + j * 256) = r[j];
        }
        bar();
        STAMP(4);
        f4 acc[8];
#pragma unroll
        for (int i = 0; i < 8; ++i) acc[i] = (f4){0.f, 0.f, 0.f, 0.f};
        __builtin_amdgcn_s_setprio(1);
        B[0] = LDB(0, 0); B[7] = LDB(7, 0); B[6] = LDB(6, 0); B[1] = LDB(1, 0); B[5] = LDB(5, 0); B[2] = LDB(2, 0); B[4] = LDB(4, 0); B[3] = LDB(3, 0);
        __builtin_amdgcn_sched_barrier(0);
#define IX(j, r) ((j) < 2 ? (j) + 8 * ((r) & 1) : (j))
#define MM(a, bb) do { acc[(a) + (bb)] = __builtin_amdgcn_mfma_f32_16x16x32_bf16(A[IX(a, r)], B[IX(bb, r)], acc[(a) + (bb)], 0, 0, 0); __builtin_amdgcn_sched_barrier(0); } while (0)
#define NA(j) do { if (r < 7) A[IX(j, r + 1)] = LDA(j, r + 1); __builtin_amdgcn_sched_barrier(0); } while (0)
#define NB(j) do { if (r < 7) B[IX(j, r + 1)] = LDB(j, r + 1); __builtin_amdgcn_sched_barrier(0); } while (0)
#pragma unroll
        for (int r = 0; r < 8; ++r) {
            MM(7, 0); NA(0); MM(0, 7); NB(0);
            MM(6, 0); NA(7); MM(0, 6); NB(7); MM(6, 1); NA(1); MM(1, 6); NB(1);
            MM(5, 0); NA(6); MM(0, 5); NB(6); MM(5, 1); MM(1, 5); MM(5, 2); MM(2, 5);
            MM(4, 0); NA(5); MM(0, 4); NB(5); MM(4, 1); MM(1, 4); MM(4, 2); MM(2, 4); MM(4, 3); MM(3, 4);
            MM(3, 0); NA(4); MM(0, 3); NB(4); MM(3, 1); MM(1, 3); MM(3, 2); MM(2, 3); MM(3, 3);
            MM(2, 0); NA(3); MM(0, 2); NB(3); MM(2, 1); MM(1, 2); MM(2, 2);
            MM(1, 0); NA(2); MM(0, 1); NB(2); MM(1, 1);
            MM(0, 0);
        }
        __builtin_amdgcn_s_setprio(0);
        STAMP(5);
#pragma unroll
        for (int i = 0; i < 8; ++i) {
            LAS u32* d = (LAS u32*)(cbase + (64 * i + 4 * col + q4) * 4);
            d[0] = pk(acc[i].x, acc[i].z);
            d[512] = pk(acc[i].y, acc[i].w);
        }
        bar();
        {
            const LAS char* tb = lds + (col >> 2) * CHS + (16 * q4 + 4 * (col & 3)) * 2;
#pragma unroll
            for (int it = 0; it < 4; ++it) {
                const int P0 = (256 * w + 64 * it + 64 * b) & 1023;
                const s4 e = __builtin_amdgcn_ds_read_tr16_b64_v4i16((LAS s4*)(tb + P0 * 2));
                const s4 o = __builtin_amdgcn_ds_read_tr16_b64_v4i16((LAS s4*)(tb + P0 * 2 + 2048));
                const v2u e2 = __builtin_bit_cast(v2u, e), o2 = __builtin_bit_cast(v2u, o);
                const v4u y = {san(e2.x), san(e2.y), san(o2.x), san(o2.y)};
                const int P = P0 + lane;
                __builtin_amdgcn_raw_buffer_store_b128(y, wr, W_Y + (P >> 2) * YS + b * 64 + (P & 3) * 16, 0, SC1);
            }
        }
        STAMP(6);
        bar();
    } else {
        const int jm = ab - 36;
        v4u mw[20]; u32 cw[20];
        flagK();
        bar();
        wait_until(now() + DLY_W);
        if (jm >= 0) {
#pragma unroll
            for (int i = 0; i < 20; ++i) {
                const int row = 32 * i + (ct >> 3);
                mw[i] = *(const v4u*)(p.wm + row * (6 * D_) + 64 * jm + 8 * (ct & 7));
                cw[i] = p.c[row];
            }
        }
        if (jm >= 0) {
            f2 a[4] = {{0.f, 0.f}, {0.f, 0.f}, {0.f, 0.f}, {0.f, 0.f}};
#pragma unroll
            for (int i = 0; i < 20; ++i) {
                const float cv = bflo(cw[i]);
                const float s = bflo(pk(cv * __builtin_amdgcn_rcpf(1.f + __expf(-cv)), 0.f));
                a[0] += bfup(mw[i].x) * s; a[1] += bfup(mw[i].y) * s; a[2] += bfup(mw[i].z) * s; a[3] += bfup(mw[i].w) * s;
            }
            float r0 = add16(add32(a[0].x, a[2].x), add32(a[1].x, a[3].x)), r1 = add16(add32(a[0].y, a[2].y), add32(a[1].y, a[3].y));
            r0 += dppf<DPP_ROR8>(r0); r1 += dppf<DPP_ROR8>(r1);
            if ((lane & 8) == 0) { f2 r = {r0, r1}; *(LAS f2*)(lds + L_MODP + w * 256 + (8 * (lane & 7) + 2 * q4) * 4) = r; }
            asm volatile("s_waitcnt lgkmcnt(0)" ::: "memory");
            u32 old = 0;
            if (lane == 0) old = __hip_atomic_fetch_add(ctl + 4, 1u, __ATOMIC_RELAXED, __HIP_MEMORY_SCOPE_WORKGROUP);
            if (__builtin_amdgcn_readfirstlane(old) == 3) {
                CFENCE();
                if (lane < 32) {
                    const LAS f2* q = (const LAS f2*)(lds + L_MODP) + lane;
                    __builtin_amdgcn_raw_buffer_store_b32(pk2((q[0] + q[32]) + (q[64] + q[96])), wr, W_MOD + jm * 128 + lane * 4, 0, SC1);
                }
                DRAIN();
                if (lane < 8) __hip_atomic_store(wsq + W_FLAGM / 8 + lane * 64 + jm, epoch, RLX);
            }
        }
        STAMP(1);
        if (isP3) {
            bf8 wf[20];
#pragma unroll
            for (int i = 0; i < 20; ++i) {
                const int row = 160 * w + 8 * i + (lane >> 3), sg = lane & 7;
                const u16* src = (sg < 4 ? p.wg : p.wu) + row * F_ + 32 * ab + 8 * (sg & 3);
                __builtin_amdgcn_global_load_lds((const GAS void*)src, (LAS void*)(lds + w * 20480 + i * 1024), 16, 0, 0);
            }
            DRAIN();
            const LAS char* tb = lds + w * 20480 + (8 * q4 + (col >> 2)) * 128 + (col & 3) * 8;
#pragma unroll
            for (int ks = 0; ks < 5; ++ks)
#pragma unroll
                for (int tl = 0; tl < 4; ++tl) {
                    const s4 lo = __builtin_amdgcn_ds_read_tr16_b64_v4i16((LAS s4*)(tb + ks * 4096 + tl * 32));
                    const s4 hi = __builtin_amdgcn_ds_read_tr16_b64_v4i16((LAS s4*)(tb + ks * 4096 + tl * 32 + 512));
                    const v2u l2 = __builtin_bit_cast(v2u, lo), h2 = __builtin_bit_cast(v2u, hi);
                    const v4u f = {l2.x, l2.y, h2.x, h2.y};
                    wf[ks * 4 + tl] = __builtin_bit_cast(bf8, f);
                }
            STAMP(2); top2();
            WINNERS();
            v4u hb[5];
            const u32 off = (col == 0 ? row1 : row2) + (160 * w + 8 * q4) * 2;
            for (;;) {
                CFENCE();
                bool ok = true;
#pragma unroll
                for (int i = 0; i < 5; ++i) {
                    hb[i] = z4;
                    if (col < 2) hb[i] = __builtin_amdgcn_raw_buffer_load_b128(wr, off + i * 64, 0, SC1);
                }
#pragma unroll
                for (int i = 0; i < 5; ++i) ok &= clean4(hb[i]);
                if (__all(ok) || now() > tmo) break;
            }
            STAMP(10);
            f4 acc[4];
#pragma unroll
            for (int tl = 0; tl < 4; ++tl) {
                acc[tl] = (f4){0.f, 0.f, 0.f, 0.f};
#pragma unroll
                for (int ks = 0; ks < 5; ++ks) acc[tl] = __builtin_amdgcn_mfma_f32_16x16x32_bf16(wf[ks * 4 + tl], __builtin_bit_cast(bf8, hb[ks]), acc[tl], 0, 0, 0);
                if (col < 2) *(LAS f4*)(lds + L_RED + w * 512 + col * 256 + tl * 64 + q4 * 16) = acc[tl];
            }
            bar();
            if (ct < 32) {
                const LAS f2* q = (const LAS f2*)(lds + L_RED + (ct >> 4) * 256 + (ct & 15) * 8);
                const f2 g = rnd((q[0] + q[64]) + (q[128] + q[192])), u = rnd((q[16] + q[80]) + (q[144] + q[208]));
                const f2 sl = {g.x * __builtin_amdgcn_rcpf(1.f + __expf(-g.x)), g.y * __builtin_amdgcn_rcpf(1.f + __expf(-g.y))};
                __builtin_amdgcn_raw_buffer_store_b32(san(pk2(rnd(sl) * u)), wr, W_A + ab * 128 + ct * 4, 0, SC1);
            }
            STAMP(11);
        } else if (isP4) {
            bf8 wf[14];
            const int ks0 = 14 * w - (w > 2 ? 1 : 0);
#pragma unroll
            for (int i = 0; i < 14; ++i) {
                const int ks = ks0 + i < 54 ? ks0 + i : 53;
                const u16* src = p.wd + (32 * ks + (lane >> 1)) * D_ + 16 * (ab - 54) + 8 * (lane & 1);
                __builtin_amdgcn_global_load_lds((const GAS void*)src, (LAS void*)(lds + w * 14336 + i * 1024), 16, 0, 0);
            }
            DRAIN();
            const LAS char* tb = lds + w * 14336 + (8 * q4 + (col >> 2)) * 32 + (col & 3) * 8;
#pragma unroll
            for (int i = 0; i < 14; ++i) {
                const s4 lo = __builtin_amdgcn_ds_read_tr16_b64_v4i16((LAS s4*)(tb + i * 1024));
                const s4 hi = __builtin_amdgcn_ds_read_tr16_b64_v4i16((LAS s4*)(tb + i * 1024 + 128));
                const v2u l2 = __builtin_bit_cast(v2u, lo), h2 = __builtin_bit_cast(v2u, hi);
                const v4u f = {l2.x, l2.y, h2.x, h2.y};
                wf[i] = __builtin_bit_cast(bf8, f);
            }
            STAMP(2); top2();
            WINNERS();
            const u64 tw = now() + DLY_D;
            const int d = 16 * (ab - 54) + 2 * (ct & 7);
            u32 xw = 0, hw = 0, gw = 0;
            const u32 xoff = ((ct >> 3) & 1 ? row2 : row1) + 1280 + d * 2;
            if (w == 0) gw = __builtin_amdgcn_raw_buffer_load_b32(wr, W_MOD + (5 * D_ + d) * 2, 0, SC1);
            wait_until(tw);
            STAMP(10);
            const int nks = w < 2 ? 14 : 13;
            v4u av[14];
            for (;;) {
                CFENCE();
                if (w == 0) { xw = __builtin_amdgcn_raw_buffer_load_b32(wr, xoff, 0, SC1); hw = __builtin_amdgcn_raw_buffer_load_b32(wr, xoff - 1280, 0, SC1); }
#pragma unroll
                for (int i = 0; i < 14; ++i) {
                    av[i] = z4;
                    if (col < 2 && i < nks) av[i] = __builtin_amdgcn_raw_buffer_load_b128(wr, W_A + (ks0 + i) * 128 + col * 64 + q4 * 16, 0, SC1);
                }
                bool ok = xw != POISON && hw != POISON;
#pragma unroll
                for (int i = 0; i < 14; ++i) ok &= clean4(av[i]);
                if (__all(ok) || now() > tmo) break;
            }
            STAMP(11);
            f4 a0 = {0.f, 0.f, 0.f, 0.f}, a1 = {0.f, 0.f, 0.f, 0.f};
#pragma unroll
            for (int i = 0; i < 14; i += 2) {
                a0 = __builtin_amdgcn_mfma_f32_16x16x32_bf16(wf[i], __builtin_bit_cast(bf8, av[i]), a0, 0, 0, 0);
                a1 = __builtin_amdgcn_mfma_f32_16x16x32_bf16(wf[i + 1], __builtin_bit_cast(bf8, av[i + 1]), a1, 0, 0, 0);
            }
            if (col < 2) *(LAS f4*)(lds + L_RED + w * 128 + col * 64 + q4 * 16) = a0 + a1;
            bar();
            if (ct < 16) {
                const LAS f2* q = (const LAS f2*)(lds + L_RED + (ct >> 3) * 64 + (ct & 7) * 8);
                const f2 y = (q[0] + q[16]) + (q[32] + q[48]);
                const u32 key = (ct >> 3) ? g2 : g1;
                u32 u = (key & ~0x7FFu) | 0x400u;
                u = (u & 0x80000000u) ? (u & 0x7FFFFFFFu) : ~u;
                const float sg = rnd1(__builtin_amdgcn_rcpf(1.f + __expf(-rnd1(__uint_as_float(u)))));
                const f2 o = rnd(bfup(gw) * rnd(bfup(hw) + rnd(rnd(y) * sg))) + bfup(xw);
                __hip_atomic_store((GAS u32*)(p.out + ((ct >> 3) ? t2 : t1) * D_ + d), pk2(o), RLX);
            }
            STAMP(12);
        }
        else { bar(); bar(); }
    }
}

extern "C" void kernel_launch(void* const* d_in, const int* in_sizes, int n_in,
                              void* d_out, int out_size, void* d_ws, size_t ws_size,
                              hipStream_t stream) {
    Params p;
    p.x = (const u16*)d_in[0]; p.k = (const u16*)d_in[1]; p.v = (const u16*)d_in[2];
    p.sh = (const u16*)d_in[3]; p.sk = (const u16*)d_in[4]; p.sD = (const u16*)d_in[5];
    p.wr = (const u16*)d_in[7]; p.wg = (const u16*)d_in[8]; p.wu = (const u16*)d_in[9]; p.wd = (const u16*)d_in[10];
    p.c = (const u16*)d_in[11]; p.wm = (const u16*)d_in[12];
    p.out = (u16*)d_out; p.ws = (char*)d_ws;
    fused_kernel<<<256, 576, 0, stream>>>(p);
}
```

```cpp
#define PRIO
#define DIAG 99
#define DIAG_ROLE 0
#define DIAG_B 0
#include <hip/hip_runtime.h>
#include <stdint.h>

typedef unsigned short u16;
typedef unsigned u32;
typedef unsigned long long u64;
typedef u32 v2u __attribute__((ext_vector_type(2)));
typedef u32 v4u __attribute__((ext_vector_type(4)));
typedef float f2 __attribute__((ext_vector_type(2)));
typedef float f4 __attribute__((ext_vector_type(4)));
typedef __bf16 bf2 __attribute__((ext_vector_type(2)));
typedef __bf16 bf8 __attribute__((ext_vector_type(8)));
typedef short s4 __attribute__((ext_vector_type(4)));
#define LAS __attribute__((address_space(3)))
#define GAS __attribute__((address_space(1)))
#define RLX __ATOMIC_RELAXED, __HIP_MEMORY_SCOPE_AGENT
#define SC1 16
#define POISON 0xFFFFFFFFu
#define OUT_AUX 16

constexpr int S_ = 2048, D_ = 640, F_ = 1728;
constexpr u32 W_EPOCH = 0;
constexpr u32 W_FLAGK = 4096;
constexpr u32 W_HINT = 12288;
constexpr u32 W_MOD = 24576;
constexpr u32 W_R = 32768;
constexpr u32 RS = 2048 + 256;
constexpr u32 W_A = 49152;
constexpr u32 W_FLAGM = 57344;
constexpr u32 W_FLAGP = 65536;
constexpr u32 W_ROWS = 131072;
constexpr u32 W_KVT = 2u << 20;
constexpr u32 YS = 10240;
constexpr u32 W_Y = 5u << 20;
constexpr u32 W_END = 8u << 20;
constexpr u32 W_STAMP = 16u << 20;
constexpr u32 CHS = 21312, KCS = 4160, L_R = 16640;
constexpr u32 L_CTL = 4 * CHS;
constexpr u32 L_LN1 = L_CTL + 64;
constexpr u32 L_CC = L_LN1 + 512;
constexpr u32 L_P2 = L_CC + 64;
constexpr u32 L_T2 = L_P2 + 512;
constexpr u32 L_RED = L_T2 + 64;
constexpr u32 L_MODP = L_RED + 2048;
constexpr u32 L_END = L_MODP + 1024;

#define DLY_A 70
#define DLY_T 0
#define DLY_H 150
#define DLY_W 10
#define DLY_S 5
#define DLY_P 10
#define SK_POS 2
#define DLY_M 130
#define DLY_M2 130
#define DLY_Y 60
#define DLY_R 40
#define DLY_D 110

struct Params {
    const u16 *x, *k, *v, *sh, *sk, *sD, *wr, *wg, *wu, *wd, *c, *wm;
    u16* out;
    char* ws;
};

__device__ __forceinline__ float bflo(u32 w) { return __uint_as_float(w << 16); }
__device__ __forceinline__ float bfhi(u32 w) { return __uint_as_float(w & 0xFFFF0000u); }
__device__ __forceinline__ f2 bfup(u32 w) { f2 r = {bflo(w), bfhi(w)}; return r; }
__device__ __forceinline__ u32 pk(float lo, float hi) { f2 a = {lo, hi}; return __builtin_bit_cast(u32, __builtin_convertvector(a, bf2)); }
__device__ __forceinline__ u32 pk2(f2 a) { return __builtin_bit_cast(u32, __builtin_convertvector(a, bf2)); }
__device__ __forceinline__ f2 rnd(f2 a) { return bfup(pk2(a)); }
__device__ __forceinline__ float rnd1(float a) { return bflo(pk(a, a)); }
__device__ __forceinline__ u32 san(u32 w) { return w < 0xFFFFFFFEu ? w : 0xFFFFFFFEu; }
__device__ __forceinline__ u32 umax(u32 a, u32 b) { return a > b ? a : b; }
__device__ __forceinline__ u32 umin(u32 a, u32 b) { return a < b ? a : b; }
__device__ __forceinline__ bool clean4(v4u d) { return umax(umax(d.x, d.y), umax(d.z, d.w)) != POISON; }
__device__ __forceinline__ u64 now() { return __builtin_amdgcn_s_memrealtime(); }
template <int C> __device__ __forceinline__ u32 dppu(u32 x) { return (u32)__builtin_amdgcn_mov_dpp((int)x, C, 0xf, 0xf, true); }
template <int C> __device__ __forceinline__ float dppf(float x) { return __uint_as_float(dppu<C>(__float_as_uint(x))); }
constexpr int DPP_X1 = 0xB1, DPP_X2 = 0x4E, DPP_HM = 0x141, DPP_M = 0x140, DPP_ROR4 = 0x124, DPP_ROR8 = 0x128;
__device__ __forceinline__ float add32(float a, float b) {
    auto s = __builtin_amdgcn_permlane32_swap(__float_as_uint(a), __float_as_uint(b), false, false);
    return __uint_as_float(s[0]) + __uint_as_float(s[1]);
}
__device__ __forceinline__ float add16(float a, float b) {
    auto s = __builtin_amdgcn_permlane16_swap(__float_as_uint(a), __float_as_uint(b), false, false);
    return __uint_as_float(s[0]) + __uint_as_float(s[1]);
}
__device__ __forceinline__ void top2_merge(u32& a1, u32& a2, u32 b1, u32 b2) {
    u32 t = umin(a1, b1), u = umax(a2, b2);
    a1 = umax(a1, b1); a2 = umax(t, u);
}
__device__ __forceinline__ void wait_until(u64 t) { while (now() < t) __builtin_amdgcn_s_sleep(1); }
__device__ __forceinline__ void bar() { asm volatile("s_waitcnt lgkmcnt(0)\n\ts_barrier" ::: "memory"); }
#define DRAIN() asm volatile("s_waitcnt vmcnt(0)" ::: "memory")
#define CFENCE() asm volatile("" ::: "memory")
#define TMO 300000ull

#define STAMP(i) do { if ((i) == DIAG) { CFENCE(); dg = now(); CFENCE(); } } while (0)

__global__ void __launch_bounds__(576) fused_kernel(Params p) {
    __shared__ __attribute__((aligned(16))) char smem[L_END];
    LAS char* lds = (LAS char*)smem;
    const int tid = threadIdx.x, b = blockIdx.x, lane = tid & 63;
    const int wave = __builtin_amdgcn_readfirstlane(tid >> 6);
    const u64 tstart = now(), tmo = tstart + TMO;
    u64 dg = tstart;
    const __amdgpu_buffer_rsrc_t wr = __builtin_amdgcn_make_buffer_rsrc(p.ws, 0, W_END, 0x00020000);
    GAS u64* const wsq = (GAS u64*)p.ws;
    LAS u32* const ctl = (LAS u32*)(lds + L_CTL);
    const u64 e0 = *(const __attribute__((address_space(4))) u64*)(uintptr_t)p.ws;
    const u64 epoch = e0 * 0xD1342543DE82EF95ull + 0x9E3779B97F4A7C15ull;
    const int tt = 16 * (b & 7) + ((b >> 3) & 15), hf = b >> 7;
    const int ab = b - 160;

    if (wave < 5) {
        const int t0 = 16 * tt + 8 * (lane >> 5), ch = 320 * hf + 64 * wave + 2 * (lane & 31);
        u32 kw[11], vw[8];
        asm volatile("" :: "s"(p.k), "s"(p.v));
        STAMP(13);
#pragma unroll
        for (int i = 0; i < 11; ++i) { int t = t0 - 3 + i; kw[i] = *(const u32*)(p.k + (t < 0 ? 0 : t) * D_ + ch); }
#pragma unroll
        for (int i = 0; i < 8; ++i) vw[i] = *(const u32*)(p.v + (t0 + i) * D_ + ch);
        const v4u sw = *(const v4u*)(p.sh + ch * 4);
        {
            if (t0 == 0) { kw[0] = 0; kw[1] = 0; kw[2] = 0; }
            const f2 s0 = {bflo(sw.x), bflo(sw.z)}, s1 = {bfhi(sw.x), bfhi(sw.z)}, s2 = {bflo(sw.y), bflo(sw.w)}, s3 = {bfhi(sw.y), bfhi(sw.w)};
            f2 kf[11], u[8];
#pragma unroll
            for (int i = 0; i < 11; ++i) kf[i] = bfup(kw[i]);
#pragma unroll
            for (int i = 0; i < 8; ++i) u[i] = (kf[i] * s3 + kf[i + 1] * s2 + kf[i + 2] * s1 + kf[i + 3] * s0) * bfup(vw[i]);
            v4u c0 = {san(pk(u[7].x, u[6].x)), san(pk(u[5].x, u[4].x)), san(pk(u[3].x, u[2].x)), san(pk(u[1].x, u[0].x))};
            v4u c1 = {san(pk(u[7].y, u[6].y)), san(pk(u[5].y, u[4].y)), san(pk(u[3].y, u[2].y)), san(pk(u[1].y, u[0].y))};
#pragma unroll
            for (int i = 0; i < 4; ++i) { auto t = __builtin_amdgcn_permlane16_swap(c0[i], c1[i], false, false); c0[i] = t[0]; c1[i] = t[1]; }
            const u32 off = W_KVT + (80 * hf + 16 * wave + ((lane & 15) >> 1)) * 16384 + (255 - (t0 >> 3)) * 64 + (2 * (lane & 1) + ((lane >> 4) & 1)) * 16;
            asm volatile("" : "+v"(c0), "+v"(c1) : "s"(epoch), "v"(off));
            STAMP(1);
            bar();
            __builtin_amdgcn_raw_buffer_store_b128(c0, wr, off, 0, SC1);
            __builtin_amdgcn_raw_buffer_store_b128(c1, wr, off + 8 * 16384, 0, SC1);
        }
        STAMP(2);
        auto poison2 = [&]() __attribute__((always_inline)) {
            const v4u ps = {POISON, POISON, POISON, POISON};
            __builtin_amdgcn_raw_buffer_store_b128(ps, wr, W_Y + b * YS + tid * 16, 0, SC1);
            __builtin_amdgcn_raw_buffer_store_b128(ps, wr, W_Y + b * YS + 5120 + tid * 16, 0, SC1);
            __builtin_amdgcn_raw_buffer_store_b128(ps, wr, W_ROWS + b * 5120 + tid * 16, 0, SC1);
            if (tid < 4) { const v2u p2 = {POISON, POISON}; __builtin_amdgcn_raw_buffer_store_b64(p2, wr, W_R + tid * RS + b * 8, 0, SC1); }
            if (b >= 160 && b < 214 && tid >= 64 && tid < 72) __builtin_amdgcn_raw_buffer_store_b128(ps, wr, W_A + (b - 160) * 128 + (tid - 64) * 16, 0, SC1);
        };
        if (b < 160) {
            bar();
            if (b < 96 && tid == 0) __hip_atomic_store(wsq + W_HINT / 8 + b * 16, epoch, RLX);
        } else if (DLY_T) wait_until(now() + DLY_T);
        const u64 tM = now() + DLY_M;
        STAMP(11);
        const int tp = lane & 3, cb = lane >> 2;
        const int tokE = 8 * b + 2 * tp;
        v2u xw[2][2];
#pragma unroll
        for (int i = 0; i < 2; ++i)
#pragma unroll
            for (int o = 0; o < 2; ++o) xw[o][i] = *(const v2u*)(p.x + (tokE + o) * D_ + 64 * (2 * wave + i) + 4 * cb);
        v2u wrw[2];
#pragma unroll
        for (int i = 0; i < 2; ++i) wrw[i] = *(const v2u*)(p.wr + 64 * (2 * wave + i) + 4 * cb);
        poison2();
        DRAIN();
        {
            u32 old = 0;
            if (lane == 0) old = __hip_atomic_fetch_add(ctl + 1, 1u, __ATOMIC_RELAXED, __HIP_MEMORY_SCOPE_WORKGROUP);
            if (__builtin_amdgcn_readfirstlane(old) == 4 && lane < 8) __hip_atomic_store(wsq + W_FLAGP / 8 + lane * 256 + b, epoch, RLX);
        }
        STAMP(3);
        f2 xf[2][4];
#pragma unroll
        for (int o = 0; o < 2; ++o) { xf[o][0] = bfup(xw[o][0].x); xf[o][1] = bfup(xw[o][0].y); xf[o][2] = bfup(xw[o][1].x); xf[o][3] = bfup(xw[o][1].y); }
        const int tl = 2 * (lane & 3) + ((lane >> 2) & 1);
        float mE, mO, rE, rO;
        {
            float s1[2], s2[2];
#pragma unroll
            for (int o = 0; o < 2; ++o) {
                f2 a = (xf[o][0] + xf[o][1]) + (xf[o][2] + xf[o][3]);
                f2 q = xf[o][0] * xf[o][0] + xf[o][1] * xf[o][1] + xf[o][2] * xf[o][2] + xf[o][3] * xf[o][3];
                s1[o] = a.x + a.y; s2[o] = q.x + q.y;
            }
            float q12 = add16(add32(s1[0], s1[1]), add32(s2[0], s2[1]));
            q12 += dppf<DPP_ROR8>(q12); q12 += dppf<DPP_ROR4>(q12);
            if ((lane & 12) == 0) *(LAS float*)(lds + L_LN1 + (2 * tp + (lane >> 5)) * 64 + (((lane >> 4) & 1) * 8 + wave) * 4) = q12;
            if (wave == 0) {
                wait_until(b < 160 ? tM : tM + DLY_M2);
                while (!__all(__hip_atomic_load(wsq + W_FLAGM / 8 + (b & 7) * 64 + (lane < 60 ? lane : 0), RLX) == epoch) && now() < tmo) __builtin_amdgcn_s_sleep(8);
            }
            asm volatile("s_waitcnt lgkmcnt(0)" ::: "memory");
            if (lane == 0) __hip_atomic_fetch_add(ctl + 2, 1u, __ATOMIC_RELAXED, __HIP_MEMORY_SCOPE_WORKGROUP);
            while (__hip_atomic_load(ctl + 2, __ATOMIC_RELAXED, __HIP_MEMORY_SCOPE_WORKGROUP) < 5u) __builtin_amdgcn_s_sleep(1);
            CFENCE();
            const LAS float* q = (const LAS float*)(lds + L_LN1 + tl * 64);
            float a = ((q[0] + q[1]) + (q[2] + q[3])) + q[4], c = ((q[8] + q[9]) + (q[10] + q[11])) + q[12];
            float m = a * (1.f / D_);
            const float rs = __builtin_amdgcn_rcpf(rnd1(__builtin_amdgcn_sqrtf(rnd1(rnd1(fmaxf(c * (1.f / D_) - m * m, 0.f)) + 1e-5f))));
            m = rnd1(m);
            float m2 = dppf<DPP_ROR4>(m), rs2 = dppf<DPP_ROR4>(rs);
            const bool od = (lane >> 2) & 1;
            mE = od ? m2 : m; mO = od ? m : m2; rE = od ? rs2 : rs; rO = od ? rs : rs2;
        }
        STAMP(4);
        f2 gh[2][4], cw[4], c1b[4], sb[4], gb[4];
        float C1, C2;
        {
            v2u mw[6][2];
#pragma unroll
            for (int j = 0; j < 6; ++j)
#pragma unroll
                for (int i = 0; i < 2; ++i) mw[j][i] = __builtin_amdgcn_raw_buffer_load_b64(wr, W_MOD + (j * D_ + 64 * (2 * wave + i) + 4 * cb) * 2, 0, SC1);
            f2 c1 = {0.f, 0.f}, c2 = {0.f, 0.f};
#pragma unroll
            for (int j = 0; j < 4; ++j) {
                const int i = j >> 1;
#define PICK(a) ((j & 1) ? (a)[i].y : (a)[i].x)
                const f2 sa = bfup(PICK(mw[0])), ca = rnd(bfup(PICK(mw[1])) + 1.f), ga = bfup(PICK(mw[2])), w = bfup(PICK(wrw));
                sb[j] = bfup(PICK(mw[3])); c1b[j] = rnd(bfup(PICK(mw[4])) + 1.f); gb[j] = bfup(PICK(mw[5]));
#undef PICK
                gh[0][j] = ga * rnd(rnd(rnd(rnd(xf[0][j] - mE) * rE) * ca) + sa);
                gh[1][j] = ga * rnd(rnd(rnd(rnd(xf[1][j] - mO) * rO) * ca) + sa);
                cw[j] = c1b[j] * w;
                c1 += cw[j]; c2 += sb[j] * w;
            }
            float q = add32(c1.x + c1.y, c2.x + c2.y);
            q = add16(q, q);
            q += dppf<DPP_ROR8>(q); q += dppf<DPP_ROR4>(q);
            if ((lane & 31) == 0) *(LAS float*)(lds + L_CC + (lane >> 5) * 32 + wave * 4) = q;
            asm volatile("s_waitcnt lgkmcnt(0)" ::: "memory");
        }
        STAMP(5);
        if (wave == 0) {
            for (;;) {
                bool ok = true;
#pragma unroll
                for (int j = 0; j < 4; ++j) ok &= __hip_atomic_load(wsq + W_FLAGP / 8 + (b & 7) * 256 + 64 * j + lane, RLX) == epoch;
                if (__all(ok) || now() > tmo) break;
                __builtin_amdgcn_s_sleep(8);
            }
            if (b == 0 && lane == 0) __hip_atomic_store(wsq + W_EPOCH / 8, epoch, RLX);
        } else if (wave == 1 && b >= 160) {
            while (__hip_atomic_load(wsq + W_HINT / 8 + ab * 16, RLX) != epoch && now() < tmo) __builtin_amdgcn_s_sleep(4);
            wait_until(now() + DLY_H);
        }
        STAMP(12);
        bar();
        {
            const LAS float* q = (const LAS float*)(lds + L_CC);
            C1 = ((q[0] + q[1]) + (q[2] + q[3])) + q[4]; C2 = ((q[8] + q[9]) + (q[10] + q[11])) + q[12];
        }
        wait_until(now() + DLY_Y);
        STAMP(6);
        v4u yw[2];
        for (;;) {
            CFENCE();
            yw[0] = __builtin_amdgcn_raw_buffer_load_b128(wr, W_Y + b * YS + wave * 2048 + lane * 16, 0, SC1);
            yw[1] = __builtin_amdgcn_raw_buffer_load_b128(wr, W_Y + b * YS + wave * 2048 + 1024 + lane * 16, 0, SC1);
            if (__all(clean4(yw[0]) && clean4(yw[1])) || now() > tmo) break;
        }
        STAMP(7);
        f2 x1[2][4];
        x1[0][0] = gh[0][0] * bfup(yw[0].x) + xf[0][0]; x1[0][1] = gh[0][1] * bfup(yw[0].y) + xf[0][1];
        x1[1][0] = gh[1][0] * bfup(yw[0].z) + xf[1][0]; x1[1][1] = gh[1][1] * bfup(yw[0].w) + xf[1][1];
        x1[0][2] = gh[0][2] * bfup(yw[1].x) + xf[0][2]; x1[0][3] = gh[0][3] * bfup(yw[1].y) + xf[0][3];
        x1[1][2] = gh[1][2] * bfup(yw[1].z) + xf[1][2]; x1[1][3] = gh[1][3] * bfup(yw[1].w) + xf[1][3];
        {
            float s1[2], s2[2], s3[2];
#pragma unroll
            for (int o = 0; o < 2; ++o) {
                f2 a = (x1[o][0] + x1[o][1]) + (x1[o][2] + x1[o][3]);
                f2 q = x1[o][0] * x1[o][0] + x1[o][1] * x1[o][1] + x1[o][2] * x1[o][2] + x1[o][3] * x1[o][3];
                f2 r = x1[o][0] * cw[0] + x1[o][1] * cw[1] + x1[o][2] * cw[2] + x1[o][3] * cw[3];
                s1[o] = a.x + a.y; s2[o] = q.x + q.y; s3[o] = r.x + r.y;
            }
            float q12 = add16(add32(s1[0], s1[1]), add32(s2[0], s2[1]));
            float q3 = add32(s3[0], s3[1]); q3 = add16(q3, q3);
            q12 += dppf<DPP_ROR8>(q12); q3 += dppf<DPP_ROR8>(q3);
            q12 += dppf<DPP_ROR4>(q12); q3 += dppf<DPP_ROR4>(q3);
            if ((lane & 12) == 0) {
                LAS float* q = (LAS float*)(lds + L_P2 + (2 * tp + (lane >> 5)) * 64);
                q[((lane >> 4) & 1) * 5 + wave] = q12;
                q[10 + wave] = q3;
            }
        }
        bar();
        u32 keyE, keyO, k1, k2;
        {
            const LAS f4* q = (const LAS f4*)(lds + L_P2 + tl * 64);
            const f4 a = q[0], c = q[1], d = q[2], e = q[3];
            const float S1 = ((a.x + a.y) + (a.z + a.w)) + c.x, S2 = ((c.y + c.z) + (c.w + d.x)) + d.y, S3 = ((d.z + d.w) + (e.x + e.y)) + e.z;
            float m = S1 * (1.f / D_);
            const float rs = __builtin_amdgcn_rcpf(rnd1(__builtin_amdgcn_sqrtf(rnd1(rnd1(fmaxf(S2 * (1.f / D_) - m * m, 0.f)) + 1e-5f))));
            m = rnd1(m);
            const float r = rs * (S3 - m * C1) + C2;
            u32 u = __float_as_uint(r);
            u = (u & 0x80000000u) ? ~u : (u | 0x80000000u);
            const u32 key = san((u & ~0x7FFu) | (u32)(2047 - (8 * b + tl)));
            const u32 key2 = dppu<DPP_ROR4>(key);
            k1 = umax(key, key2); k2 = umin(key, key2);
            top2_merge(k1, k2, dppu<DPP_X1>(k1), dppu<DPP_X1>(k2));
            top2_merge(k1, k2, dppu<DPP_X2>(k1), dppu<DPP_X2>(k2));
            if (tid < 4) { const v2u kk = {k1, k2}; __builtin_amdgcn_raw_buffer_store_b64(kk, wr, W_R + tid * RS + b * 8, 0, SC1); }
            STAMP(8);
            const float m2 = dppf<DPP_ROR4>(m), rs2 = dppf<DPP_ROR4>(rs);
            const bool od = (lane >> 2) & 1;
            mE = od ? m2 : m; mO = od ? m : m2; rE = od ? rs2 : rs; rO = od ? rs : rs2;
            keyE = od ? key2 : key; keyO = od ? key : key2;
        }
        u32 ow[2][4];
#pragma unroll
        for (int o = 0; o < 2; ++o) {
            const float m = o ? mO : mE, rs = o ? rO : rE;
            const u32 key = o ? keyO : keyE;
            u32 hw[4];
#pragma unroll
            for (int j = 0; j < 4; ++j) {
                hw[j] = san(pk2(rnd(rnd(rnd(x1[o][j] - m) * rs) * c1b[j]) + sb[j]));
                ow[o][j] = pk2(gb[j] * bfup(hw[j]) + x1[o][j]);
            }
            if (key == k1 || key == k2) {
                const u32 off = W_ROWS + b * 5120 + (key == k1 ? 0 : 2560) + (128 * wave + 4 * cb) * 2;
                v2u a = {hw[0], hw[1]}, c = {hw[2], hw[3]};
                __builtin_amdgcn_raw_buffer_store_b64(a, wr, off, 0, SC1);
                __builtin_amdgcn_raw_buffer_store_b64(c, wr, off + 128, 0, SC1);
            }
        }
        STAMP(9);
        {
            const __amdgpu_buffer_rsrc_t orr = __builtin_amdgcn_make_buffer_rsrc(p.out, 0, S_ * D_ * 2, 0x00020000);
#pragma unroll
            for (int o = 0; o < 2; ++o) {
                const u32 off = ((tokE + o) * D_ + 128 * wave + 4 * cb) * 2;
                v2u a0 = {ow[o][0], ow[o][1]}, a1 = {ow[o][2], ow[o][3]};
                __builtin_amdgcn_raw_buffer_store_b64(a0, orr, off, 0, SC1);
                __builtin_amdgcn_raw_buffer_store_b64(a1, orr, off + 128, 0, SC1);
            }
            DRAIN();
#pragma unroll
            for (int o = 0; o < 2; ++o) {
                const u32 key = o ? keyO : keyE;
                if (key == k1 || key == k2) {
                    const u32 off = W_ROWS + b * 5120 + (key == k1 ? 0 : 2560) + 1280 + (128 * wave + 4 * cb) * 2;
                    v2u a = {san(pk2(x1[o][0])), san(pk2(x1[o][1]))}, c = {san(pk2(x1[o][2])), san(pk2(x1[o][3]))};
                    __builtin_amdgcn_raw_buffer_store_b64(a, wr, off, 0, SC1);
                    __builtin_amdgcn_raw_buffer_store_b64(c, wr, off + 128, 0, SC1);
                }
            }
        }
        STAMP(10);
        if (DIAG_ROLE == 0 && b == DIAG_B && wave == 0) wait_until(tstart + 5000 + 4 * (dg - tstart));
        return;
    }

    const int w = wave - 5, ct = tid - 320;
    const int col = lane & 15, q4 = lane >> 4;
    const bool isP3 = ab >= 0 && ab < 54, isP4 = ab >= 54 && ab < 94;
    u32 g1, g2;
    {
        const v4u ps = {POISON, POISON, POISON, POISON};
        const u32 off = W_KVT + (80 * hf + 20 * w + (lane >> 3)) * 16384 + (254 - 2 * tt) * 64 + (lane & 7) * 16;
        asm volatile("" :: "s"(epoch), "v"(off));
        if (DLY_P) wait_until(now() + DLY_P);
        __builtin_amdgcn_raw_buffer_store_b128(ps, wr, off, 0, SC1);
        __builtin_amdgcn_raw_buffer_store_b128(ps, wr, off + 8 * 16384, 0, SC1);
        if (lane < 32) __builtin_amdgcn_raw_buffer_store_b128(ps, wr, off + 16 * 16384, 0, SC1);
        if (ct < 16) ctl[ct] = 0;
    }
    auto flagK = [&]() __attribute__((always_inline)) {
        DRAIN();
        if (lane == 0) __hip_atomic_store(wsq + W_FLAGK / 8 + (hf * 4 + w) * 128 + tt, epoch, RLX);
    };
    auto top2 = [&]() __attribute__((always_inline)) {
        bar();
        bar();
        wait_until(now() + DLY_R);
        STAMP(7);
        v4u a, c;
        for (;;) {
            CFENCE();
            a = __builtin_amdgcn_raw_buffer_load_b128(wr, W_R + w * RS + lane * 16, 0, SC1);
            c = __builtin_amdgcn_raw_buffer_load_b128(wr, W_R + w * RS + 1024 + lane * 16, 0, SC1);
            if (__all(clean4(a) && clean4(c)) || now() > tmo) break;
        }
        STAMP(8);
        g1 = a.x; g2 = a.y;
        top2_merge(g1, g2, a.z, a.w);
        u32 h1 = c.x, h2 = c.y;
        top2_merge(h1, h2, c.z, c.w);
        top2_merge(g1, g2, h1, h2);
        top2_merge(g1, g2, dppu<DPP_X1>(g1), dppu<DPP_X1>(g2));
        top2_merge(g1, g2, dppu<DPP_X2>(g1), dppu<DPP_X2>(g2));
        top2_merge(g1, g2, dppu<DPP_HM>(g1), dppu<DPP_HM>(g2));
        top2_merge(g1, g2, dppu<DPP_M>(g1), dppu<DPP_M>(g2));
        { auto s = __builtin_amdgcn_permlane16_swap(g1, g1, false, false); auto t = __builtin_amdgcn_permlane16_swap(g2, g2, false, false);
          g1 = s[0]; g2 = t[0]; top2_merge(g1, g2, s[1], t[1]); }
        { auto s = __builtin_amdgcn_permlane32_swap(g1, g1, false, false); auto t = __builtin_amdgcn_permlane32_swap(g2, g2, false, false);
          g1 = s[0]; g2 = t[0]; top2_merge(g1, g2, s[1], t[1]); }
        STAMP(9);
    };
#define WINNERS() const int t1 = 2047 - (g1 & 0x7FF), t2 = 2047 - (g2 & 0x7FF); \
    const u32 row1 = W_ROWS + (t1 >> 3) * 5120, row2 = W_ROWS + (t2 >> 3) * 5120 + ((t1 >> 3) == (t2 >> 3) ? 2560 : 0); \
    const v4u z4 = {0, 0, 0, 0};
    if (b < 160) {
        const int ch = 4 * b + w;
        LAS char* cbase = lds + w * CHS;
        v4u c1[4];
        u32 db;
        auto ldk = [&]() __attribute__((always_inline)) {
#pragma unroll
            for (int j = 0; j < 4; ++j) c1[j] = *(const v4u*)(p.sk + ch * S_ + 8 * (64 * j + lane));
            db = p.sD[ch];
        };
        if (SK_POS == 0) ldk();
        flagK();
        if (SK_POS == 1) ldk();
        STAMP(1);
        bar();
        const u64 tk = now() + DLY_A;
        if (SK_POS == 2) { if (DLY_S) wait_until(now() + DLY_S); ldk(); }
        const int fk = ((b / 80) * 4 + (b % 80) / 20) * 128 + 96 - 32 * w + (lane & 31);
        u64 fl = __hip_atomic_load(wsq + W_FLAGK / 8 + fk, RLX);
        LAS char* rbase = cbase + L_R + ((96 * w) & 127);
        {
            const v4u z4 = {0, 0, 0, 0};
            if (lane < 30) *(LAS v4u*)(rbase + 4096 + lane * 16) = z4;
            if (lane == 0) c1[0].x = (c1[0].x & 0xFFFF0000u) | (pk(bflo(c1[0].x) + bflo(db), 0.f) & 0xFFFFu);
            const u32 k0 = __builtin_amdgcn_readfirstlane(c1[0].x), k1 = __builtin_amdgcn_readfirstlane(c1[0].y);
            if (lane < 8) {
                v4u h = z4;
                if (lane == 4) h.w = k0 << 16;
                if (lane == 5) h.w = k0;
                if (lane == 6) { h.z = k0 << 16; h.w = (k0 >> 16) | (k1 << 16); }
                if (lane == 7) { h.z = k0; h.w = k1; }
                *(LAS v4u*)(cbase + (lane & 3) * KCS + (lane >> 2) * 16) = h;
            }
#pragma unroll
            for (int j = 0; j < 4; ++j) {
                const u32 n0 = j < 3 ? __builtin_amdgcn_readfirstlane(c1[j < 3 ? j + 1 : j].x) : 0u, n1 = j < 3 ? __builtin_amdgcn_readfirstlane(c1[j < 3 ? j + 1 : j].y) : 0u;
                const u32 e[6] = {c1[j].x, c1[j].y, c1[j].z, c1[j].w,
                                  (u32)__builtin_amdgcn_update_dpp((int)n0, (int)c1[j].x, 0x130, 0xf, 0xf, false), (u32)__builtin_amdgcn_update_dpp((int)n1, (int)c1[j].y, 0x130, 0xf, 0xf, false)};
                u32 o[5];
#pragma unroll
                for (int i = 0; i < 5; ++i) o[i] = __builtin_amdgcn_alignbit(e[i + 1], e[i], 16);
                LAS char* dst = cbase + (64 * j + lane + 2) * 16;
#pragma unroll
                for (int m = 0; m < 2; ++m) {
                    const v4u a = {o[m], o[m + 1], o[m + 2], o[m + 3]}, c = {e[m + 1], e[m + 2], e[m + 3], e[m + 4]};
                    *(LAS v4u*)(dst + (2 * m) * KCS) = a;
                    *(LAS v4u*)(dst + (2 * m + 1) * KCS) = c;
                }
            }
        }
        const LAS char* pa = cbase + (col & 3) * KCS + (2 * q4 + (col >> 2)) * 8;
        const LAS char* pb = rbase + 480 + 16 * q4 - 32 * col;
        const LAS char* pa2 = pa + 8;
        asm volatile("" : "+v"(pa2));
        auto lda = [&](int o) __attribute__((always_inline)) {
            const v2u lo = *(const LAS v2u*)(pa + o), hi = *(const LAS v2u*)(pa2 + o);
            const v4u f = {lo.x, lo.y, hi.x, hi.y};
            return __builtin_bit_cast(bf8, f);
        };
#define LDA(a, r) lda((a) * 512 + (r) * 64)
#define LDB(bb, r) (*(const LAS bf8*)(pb + 3584 - (bb) * 512 + (r) * 64))
        bf8 A[10], B[10];
#pragma unroll
        for (int j = 0; j < 8; ++j) A[j] = LDA(j, 0);
        while (!__all(fl == epoch) && now() < tmo) { __builtin_amdgcn_s_sleep(2); fl = __hip_atomic_load(wsq + W_FLAGK / 8 + fk, RLX); }
        STAMP(2);
        wait_until(tk);
        STAMP(3);
        {
            v4u r[4];
            for (;;) {
                CFENCE();
#pragma unroll
                for (int j = 0; j < 4; ++j) r[j] = __builtin_amdgcn_raw_buffer_load_b128(wr, W_KVT + b * 16384 + w * 4096 + j * 1024 + lane * 16, 0, SC1);
                if (__all(clean4(r[0]) && clean4(r[1]) && clean4(r[2]) && clean4(r[3])) || now() > tmo) break;
            }
            STAMP(10);
            LAS char* dst = lds + (lane & 3) * CHS + L_R + ((96 * lane) & 127) + (64 * w + (lane >> 2)) * 16;
#pragma unroll
            for (int j = 0; j < 4; ++j) *(LAS v4u*)(dst + j * 256) = r[j];
        }
        bar();
        STAMP(4);
        f4 acc[8];
#pragma unroll
        for (int i = 0; i < 8; ++i) acc[i] = (f4){0.f, 0.f, 0.f, 0.f};
        __builtin_amdgcn_s_setprio(1);
        B[0] = LDB(0, 0); B[7] = LDB(7, 0); B[6] = LDB(6, 0); B[1] = LDB(1, 0); B[5] = LDB(5, 0); B[2] = LDB(2, 0); B[4] = LDB(4, 0); B[3] = LDB(3, 0);
        __builtin_amdgcn_sched_barrier(0);
#define IX(j, r) ((j) < 2 ? (j) + 8 * ((r) & 1) : (j))
#define MM(a, bb) do { acc[(a) + (bb)] = __builtin_amdgcn_mfma_f32_16x16x32_bf16(A[IX(a, r)], B[IX(bb, r)], acc[(a) + (bb)], 0, 0, 0); __builtin_amdgcn_sched_barrier(0); } while (0)
#define NA(j) do { if (r < 7) A[IX(j, r + 1)] = LDA(j, r + 1); __builtin_amdgcn_sched_barrier(0); } while (0)
#define NB(j) do { if (r < 7) B[IX(j, r + 1)] = LDB(j, r + 1); __builtin_amdgcn_sched_barrier(0); } while (0)
#pragma unroll
        for (int r = 0; r < 8; ++r) {
            MM(7, 0); NA(0); MM(0, 7); NB(0);
            MM(6, 0); NA(7); MM(0, 6); NB(7); MM(6, 1); NA(1); MM(1, 6); NB(1);
            MM(5, 0); NA(6); MM(0, 5); NB(6); MM(5, 1); MM(1, 5); MM(5, 2); MM(2, 5);
            MM(4, 0); NA(5); MM(0, 4); NB(5); MM(4, 1); MM(1, 4); MM(4, 2); MM(2, 4); MM(4, 3); MM(3, 4);
            MM(3, 0); NA(4); MM(0, 3); NB(4); MM(3, 1); MM(1, 3); MM(3, 2); MM(2, 3); MM(3, 3);
            MM(2, 0); NA(3); MM(0, 2); NB(3); MM(2, 1); MM(1, 2); MM(2, 2);
            MM(1, 0); NA(2); MM(0, 1); NB(2); MM(1, 1);
            MM(0, 0);
        }
        __builtin_amdgcn_s_setprio(0);
        STAMP(5);
#pragma unroll
        for (int i = 0; i < 8; ++i) {
            LAS u32* d = (LAS u32*)(cbase + (64 * i + 4 * col + q4) * 4);
            d[0] = pk(acc[i].x, acc[i].z);
            d[512] = pk(acc[i].y, acc[i].w);
        }
        bar();
        {
            const LAS char* tb = lds + (col >> 2) * CHS + (16 * q4 + 4 * (col & 3)) * 2;
#pragma unroll
            for (int it = 0; it < 4; ++it) {
                const int P0 = (256 * w + 64 * it + 64 * b) & 1023;
                const s4 e = __builtin_amdgcn_ds_read_tr16_b64_v4i16((LAS s4*)(tb + P0 * 2));
                const s4 o = __builtin_amdgcn_ds_read_tr16_b64_v4i16((LAS s4*)(tb + P0 * 2 + 2048));
                const v2u e2 = __builtin_bit_cast(v2u, e), o2 = __builtin_bit_cast(v2u, o);
                const v4u y = {san(e2.x), san(e2.y), san(o2.x), san(o2.y)};
                const int P = P0 + lane;
                __builtin_amdgcn_raw_buffer_store_b128(y, wr, W_Y + (P >> 2) * YS + b * 64 + (P & 3) * 16, 0, SC1);
            }
        }
        STAMP(6);
        bar();
    } else {
        const int jm = ab - 36;
        v4u mw[20]; u32 cw[20];
        flagK();
        bar();
        wait_until(now() + DLY_W);
        if (jm >= 0) {
#pragma unroll
            for (int i = 0; i < 20; ++i) {
                const int row = 32 * i + (ct >> 3);
                mw[i] = *(const v4u*)(p.wm + row * (6 * D_) + 64 * jm + 8 * (ct & 7));
                cw[i] = p.c[row];
            }
        }
        if (jm >= 0) {
            f2 a[4] = {{0.f, 0.f}, {0.f, 0.f}, {0.f, 0.f}, {0.f, 0.f}};
#pragma unroll
            for (int i = 0; i < 20; ++i) {
                const float cv = bflo(cw[i]);
                const float s = bflo(pk(cv * __builtin_amdgcn_rcpf(1.f + __expf(-cv)), 0.f));
                a[0] += bfup(mw[i].x) * s; a[1] += bfup(mw[i].y) * s; a[2] += bfup(mw[i].z) * s; a[3] += bfup(mw[i].w) * s;
            }
            float r0 = add16(add32(a[0].x, a[2].x), add32(a[1].x, a[3].x)), r1 = add16(add32(a[0].y, a[2].y), add32(a[1].y, a[3].y));
            r0 += dppf<DPP_ROR8>(r0); r1 += dppf<DPP_ROR8>(r1);
            if ((lane & 8) == 0) { f2 r = {r0, r1}; *(LAS f2*)(lds + L_MODP + w * 256 + (8 * (lane & 7) + 2 * q4) * 4) = r; }
            asm volatile("s_waitcnt lgkmcnt(0)" ::: "memory");
            u32 old = 0;
            if (lane == 0) old = __hip_atomic_fetch_add(ctl + 4, 1u, __ATOMIC_RELAXED, __HIP_MEMORY_SCOPE_WORKGROUP);
            if (__builtin_amdgcn_readfirstlane(old) == 3) {
                CFENCE();
                if (lane < 32) {
                    const LAS f2* q = (const LAS f2*)(lds + L_MODP) + lane;
                    __builtin_amdgcn_raw_buffer_store_b32(pk2((q[0] + q[32]) + (q[64] + q[96])), wr, W_MOD + jm * 128 + lane * 4, 0, SC1);
                }
                DRAIN();
                if (lane < 8) __hip_atomic_store(wsq + W_FLAGM / 8 + lane * 64 + jm, epoch, RLX);
            }
        }
        STAMP(1);
        if (isP3) {
            bf8 wf[20];
#pragma unroll
            for (int i = 0; i < 20; ++i) {
                const int row = 160 * w + 8 * i + (lane >> 3), sg = lane & 7;
                const u16* src = (sg < 4 ? p.wg : p.wu) + row * F_ + 32 * ab + 8 * (sg & 3);
                __builtin_amdgcn_global_load_lds((const GAS void*)src, (LAS void*)(lds + w * 20480 + i * 1024), 16, 0, 0);
            }
            DRAIN();
            const LAS char* tb = lds + w * 20480 + (8 * q4 + (col >> 2)) * 128 + (col & 3) * 8;
#pragma unroll
            for (int ks = 0; ks < 5; ++ks)
#pragma unroll
                for (int tl = 0; tl < 4; ++tl) {
                    const s4 lo = __builtin_amdgcn_ds_read_tr16_b64_v4i16((LAS s4*)(tb + ks * 4096 + tl * 32));
                    const s4 hi = __builtin_amdgcn_ds_read_tr16_b64_v4i16((LAS s4*)(tb + ks * 4096 + tl * 32 + 512));
                    const v2u l2 = __builtin_bit_cast(v2u, lo), h2 = __builtin_bit_cast(v2u, hi);
                    const v4u f = {l2.x, l2.y, h2.x, h2.y};
                    wf[ks * 4 + tl] = __builtin_bit_cast(bf8, f);
                }
            STAMP(2); top2();
            WINNERS();
            v4u hb[5];
            const u32 off = (col == 0 ? row1 : row2) + (160 * w + 8 * q4) * 2;
            for (;;) {
                CFENCE();
                bool ok = true;
#pragma unroll
                for (int i = 0; i < 5; ++i) {
                    hb[i] = z4;
                    if (col < 2) hb[i] = __builtin_amdgcn_raw_buffer_load_b128(wr, off + i * 64, 0, SC1);
                }
#pragma unroll
                for (int i = 0; i < 5; ++i) ok &= clean4(hb[i]);
                if (__all(ok) || now() > tmo) break;
            }
            STAMP(10);
            f4 acc[4];
#pragma unroll
            for (int tl = 0; tl < 4; ++tl) {
                acc[tl] = (f4){0.f, 0.f, 0.f, 0.f};
#pragma unroll
                for (int ks = 0; ks < 5; ++ks) acc[tl] = __builtin_amdgcn_mfma_f32_16x16x32_bf16(wf[ks * 4 + tl], __builtin_bit_cast(bf8, hb[ks]), acc[tl], 0, 0, 0);
                if (col < 2) *(LAS f4*)(lds + L_RED + w * 512 + col * 256 + tl * 64 + q4 * 16) = acc[tl];
            }
            bar();
            if (ct < 32) {
                const LAS f2* q = (const LAS f2*)(lds + L_RED + (ct >> 4) * 256 + (ct & 15) * 8);
                const f2 g = rnd((q[0] + q[64]) + (q[128] + q[192])), u = rnd((q[16] + q[80]) + (q[144] + q[208]));
                const f2 sl = {g.x * __builtin_amdgcn_rcpf(1.f + __expf(-g.x)), g.y * __builtin_amdgcn_rcpf(1.f + __expf(-g.y))};
                __builtin_amdgcn_raw_buffer_store_b32(san(pk2(rnd(sl) * u)), wr, W_A + ab * 128 + ct * 4, 0, SC1);
            }
            STAMP(11);
        } else if (isP4) {
            bf8 wf[14];
            const int ks0 = 14 * w - (w > 2 ? 1 : 0);
#pragma unroll
            for (int i = 0; i < 14; ++i) {
                const int ks = ks0 + i < 54 ? ks0 + i : 53;
                const u16* src = p.wd + (32 * ks + (lane >> 1)) * D_ + 16 * (ab - 54) + 8 * (lane & 1);
                __builtin_amdgcn_global_load_lds((const GAS void*)src, (LAS void*)(lds + w * 14336 + i * 1024), 16, 0, 0);
            }
            DRAIN();
            const LAS char* tb = lds + w * 14336 + (8 * q4 + (col >> 2)) * 32 + (col & 3) * 8;
#pragma unroll
            for (int i = 0; i < 14; ++i) {
                const s4 lo = __builtin_amdgcn_ds_read_tr16_b64_v4i16((LAS s4*)(tb + i * 1024));
                const s4 hi = __builtin_amdgcn_ds_read_tr16_b64_v4i16((LAS s4*)(tb + i * 1024 + 128));
                const v2u l2 = __builtin_bit_cast(v2u, lo), h2 = __builtin_bit_cast(v2u, hi);
                const v4u f = {l2.x, l2.y, h2.x, h2.y};
                wf[i] = __builtin_bit_cast(bf8, f);
            }
            STAMP(2); top2();
            WINNERS();
            const u64 tw = now() + DLY_D;
            const int d = 16 * (ab - 54) + 2 * (ct & 7);
            u32 xw = 0, hw = 0, gw = 0;
            const u32 xoff = ((ct >> 3) & 1 ? row2 : row1) + 1280 + d * 2;
            if (w == 0) gw = __builtin_amdgcn_raw_buffer_load_b32(wr, W_MOD + (5 * D_ + d) * 2, 0, SC1);
            wait_until(tw);
            STAMP(10);
            const int nks = w < 2 ? 14 : 13;
            v4u av[14];
            for (;;) {
                CFENCE();
                if (w == 0) { xw = __builtin_amdgcn_raw_buffer_load_b32(wr, xoff, 0, SC1); hw = __builtin_amdgcn_raw_buffer_load_b32(wr, xoff - 1280, 0, SC1); }
#pragma unroll
                for (int i = 0; i < 14; ++i) {
                    av[i] = z4;
                    if (col < 2 && i < nks) av[i] = __builtin_amdgcn_raw_buffer_load_b128(wr, W_A + (ks0 + i) * 128 + col * 64 + q4 * 16, 0, SC1);
                }
                bool ok = xw != POISON && hw != POISON;
#pragma unroll
                for (int i = 0; i < 14; ++i) ok &= clean4(av[i]);
                if (__all(ok) || now() > tmo) break;
            }
            STAMP(11);
            f4 a0 = {0.f, 0.f, 0.f, 0.f}, a1 = {0.f, 0.f, 0.f, 0.f};
#pragma unroll
            for (int i = 0; i < 14; i += 2) {
                a0 = __builtin_amdgcn_mfma_f32_16x16x32_bf16(wf[i], __builtin_bit_cast(bf8, av[i]), a0, 0, 0, 0);
                a1 = __builtin_amdgcn_mfma_f32_16x16x32_bf16(wf[i + 1], __builtin_bit_cast(bf8, av[i + 1]), a1, 0, 0, 0);
            }
            if (col < 2) *(LAS f4*)(lds + L_RED + w * 128 + col * 64 + q4 * 16) = a0 + a1;
            bar();
            if (ct < 16) {
                const LAS f2* q = (const LAS f2*)(lds + L_RED + (ct >> 3) * 64 + (ct & 7) * 8);
                const f2 y = (q[0] + q[16]) + (q[32] + q[48]);
                const u32 key = (ct >> 3) ? g2 : g1;
                u32 u = (key & ~0x7FFu) | 0x400u;
                u = (u & 0x80000000u) ? (u & 0x7FFFFFFFu) : ~u;
                const float sg = rnd1(__builtin_amdgcn_rcpf(1.f + __expf(-rnd1(__uint_as_float(u)))));
                const f2 o = rnd(bfup(gw) * rnd(bfup(hw) + rnd(rnd(y) * sg))) + bfup(xw);
                __hip_atomic_store((GAS u32*)(p.out + ((ct >> 3) ? t2 : t1) * D_ + d), pk2(o), RLX);
            }
            STAMP(12);
        }
        else { bar(); bar(); }
    }
    if (DIAG_ROLE == 1 && b == DIAG_B && wave == 5) wait_until(tstart + 5000 + 4 * (dg - tstart));
}

extern "C" void kernel_launch(void* const* d_in, const int* in_sizes, int n_in,
                              void* d_out, int out_size, void* d_ws, size_t ws_size,
                              hipStream_t stream) {
    Params p;
    p.x = (const u16*)d_in[0]; p.k = (const u16*)d_in[1]; p.v = (const u16*)d_in[2];
    p.sh = (const u16*)d_in[3]; p.sk = (const u16*)d_in[4]; p.sD = (const u16*)d_in[5];
    p.wr = (const u16*)d_in[7]; p.wg = (const u16*)d_in[8]; p.wu = (const u16*)d_in[9]; p.wd = (const u16*)d_in[10];
    p.c = (const u16*)d_in[11]; p.wm = (const u16*)d_in[12];
    p.out = (u16*)d_out; p.ws = (char*)d_ws;
    fused_kernel<<<256, 576, 0, stream>>>(p);
}
```

```cpp
#define DLY_T1 200
#define DLY_XC 70
#define DLY_F 100
#include <hip/hip_runtime.h>
#include <stdint.h>

typedef unsigned short u16;
typedef unsigned u32;
typedef unsigned long long u64;
typedef u32 v2u __attribute__((ext_vector_type(2)));
typedef u32 v4u __attribute__((ext_vector_type(4)));
typedef float f2 __attribute__((ext_vector_type(2)));
typedef float f4 __attribute__((ext_vector_type(4)));
typedef __bf16 bf2 __attribute__((ext_vector_type(2)));
typedef __bf16 bfx8 __attribute__((ext_vector_type(8)));
typedef short s4 __attribute__((ext_vector_type(4)));
#define LAS __attribute__((address_space(3)))
#define GAS __attribute__((address_space(1)))
#define RLX __ATOMIC_RELAXED, __HIP_MEMORY_SCOPE_AGENT
#define WG __ATOMIC_RELAXED, __HIP_MEMORY_SCOPE_WORKGROUP
#define SC1 16
#define POISON 0xFFFFFFFFu
#define OUT_AUX 16

constexpr int S_ = 2048, D_ = 640, F_ = 1728;
constexpr u32 W_EPOCH = 0;
constexpr u32 W_FLAGK = 4096;
constexpr u32 W_HINT = 20480;
constexpr u32 MS = 10240 + 256;
constexpr u32 W_MOD = 294912;
constexpr u32 W_FLAGP = 65536;
constexpr u32 W_XF = 98304;
constexpr u32 W_R = 131072;
#define RSL 32
constexpr u32 RS = 256 * RSL + 256;
constexpr u32 W_A = 278528;
constexpr u32 AS = 7168;
constexpr u32 W_KVT = 2u << 20;
constexpr u32 YS = 15360;
constexpr u32 W_Y = 5u << 20;
constexpr u32 W_END = 9u << 20;
constexpr u32 W_STAMP = 16u << 20;
constexpr u32 CHS = 21312, KCS = 4160, L_R = 16640;
constexpr u32 L_CTL = 4 * CHS;
constexpr u32 L_LN1 = L_CTL + 64;
constexpr u32 L_CC = L_LN1 + 512;
constexpr u32 L_P2 = L_CC + 64;
constexpr u32 L_RED = L_P2 + 512;
constexpr u32 L_MODP = L_RED + 2048;
constexpr u32 L_MOD = L_MODP + 1024;
constexpr u32 STR = 336, STA = 8 * STR;
constexpr u32 L_STG = L_MOD + 10240;
constexpr u32 L_END = L_STG + 12 * STA;

#define DLY_A 70
#define DLY_K 45
#define DLY_T 0
#define DLY_H 265
#define DLY_W 0
#define DLY_S 5
#define DLY_P 10
#define SK_POS 3
#define DLY_M 80
#define DLY_X 200
#define DLY_XA 50
#define DLY_P2 0
#define DLY_AX 15
#define DLY_V 0
#define CODE_LINES 18
#define CODE_MAX 32768u
#define PSHIFT 1
#define ME 0
#define CODEPF 1
#define DLY_Y 350
#define Y_POS 5
#define DLY_R 500
#define DLY_R4 500
#define DLY_O 500
#define POIS_POS 0
#define DLY_Q 0
#define DLY_N 100
#define NW_W 4
#define DLY_XL 300
#define TPRIO 3
#define LN1_POS 1
#define DLY_QA 0
#define DLY_T2 260
#define W_LATE 1
#define PF_POS 0
#define NLATE 10
#define PF4_LATE 0
#define DLY_F4 15
#define DLY_W4 100
#define WL3 1
#define WL4 1
#define DLY_WL 180
#define STG_R 6
#define STG_A 6
#define DLY_YE 0
#define DLY_O3 560
#define STG_O 0
#define DLY_XF 700
#define DLY_RB 60
#define MPF 0
#define DLY_D 90

struct Params {
    const u16 *x, *k, *v, *sh, *sk, *sD, *wr, *wg, *wu, *wd, *c, *wm;
    u16* out;
    char* ws;
};

__device__ __forceinline__ float bflo(u32 w) { return __uint_as_float(w << 16); }
__device__ __forceinline__ float bfhi(u32 w) { return __uint_as_float(w & 0xFFFF0000u); }
__device__ __forceinline__ f2 bfup(u32 w) { f2 r = {bflo(w), bfhi(w)}; return r; }
__device__ __forceinline__ u32 pk(float lo, float hi) { f2 a = {lo, hi}; return __builtin_bit_cast(u32, __builtin_convertvector(a, bf2)); }
__device__ __forceinline__ u32 pk2(f2 a) { return __builtin_bit_cast(u32, __builtin_convertvector(a, bf2)); }
__device__ __forceinline__ f2 rnd(f2 a) { return bfup(pk2(a)); }
__device__ __forceinline__ float rnd1(float a) { return bflo(pk(a, a)); }
__device__ __forceinline__ u32 san(u32 w) { return w < 0xFFFFFFFEu ? w : 0xFFFFFFFEu; }
__device__ __forceinline__ u32 umax(u32 a, u32 b) { return a > b ? a : b; }
__device__ __forceinline__ u32 umin(u32 a, u32 b) { return a < b ? a : b; }
__device__ __forceinline__ bool clean4(v4u d) { return umax(umax(d.x, d.y), umax(d.z, d.w)) != POISON; }
__device__ __forceinline__ u64 now64() { return __builtin_amdgcn_s_memrealtime(); }
__device__ __forceinline__ u32 now() { return (u32)__builtin_amdgcn_s_memrealtime(); }
__device__ __forceinline__ bool past(u32 t) { return (int)(now() - t) >= 0; }
template <int C> __device__ __forceinline__ u32 dppu(u32 x) { return (u32)__builtin_amdgcn_mov_dpp((int)x, C, 0xf, 0xf, true); }
template <int C> __device__ __forceinline__ float dppf(float x) { return __uint_as_float(dppu<C>(__float_as_uint(x))); }
constexpr int DPP_X1 = 0xB1, DPP_X2 = 0x4E, DPP_HM = 0x141, DPP_M = 0x140, DPP_ROR4 = 0x124, DPP_ROR8 = 0x128;
__device__ __forceinline__ float add32(float a, float b) {
    auto s = __builtin_amdgcn_permlane32_swap(__float_as_uint(a), __float_as_uint(b), false, false);
    return __uint_as_float(s[0]) + __uint_as_float(s[1]);
}
__device__ __forceinline__ float add16(float a, float b) {
    auto s = __builtin_amdgcn_permlane16_swap(__float_as_uint(a), __float_as_uint(b), false, false);
    return __uint_as_float(s[0]) + __uint_as_float(s[1]);
}
__device__ __forceinline__ void top2_merge(u32& a1, u32& a2, u32 b1, u32 b2) {
    u32 t = umin(a1, b1), u = umax(a2, b2);
    a1 = umax(a1, b1); a2 = umax(t, u);
}
__device__ __forceinline__ void wait_until(u32 t) { while (!past(t)) __builtin_amdgcn_s_sleep(1); }
__device__ __forceinline__ void bar() { asm volatile("s_waitcnt lgkmcnt(0)\n\ts_barrier" ::: "memory"); }
#define DRAIN() asm volatile("s_waitcnt vmcnt(0)" ::: "memory")
#define CFENCE() asm volatile("" ::: "memory")
#define TMO 50000000u

#define STAMP(i) do {} while (0)

#define SVC(i) ((i) == 3 || (i) == 12)
#define STAMPX(i) do {} while (0)
__global__ void __launch_bounds__(576) fused_kernel(Params p) {
    __shared__ __attribute__((aligned(16))) char smem[L_END];
    LAS char* lds = (LAS char*)smem;
    const int tid = threadIdx.x, b = blockIdx.x, lane = tid & 63;
    const int wave = __builtin_amdgcn_readfirstlane(tid >> 6);
    asm volatile("" :: "s"(p.k), "s"(p.v), "s"(p.sh), "s"(p.sk), "s"(p.sD), "s"(p.ws), "s"(p.x), "s"(p.wm));
    u64 pc0;
    asm volatile("s_getpc_b64 %0" : "=s"(pc0));
    const __amdgpu_buffer_rsrc_t wr = __builtin_amdgcn_make_buffer_rsrc(p.ws, 0, W_END, 0x00020000);
    GAS u64* const wsq = (GAS u64*)p.ws;
    LAS u32* const ctl = (LAS u32*)(lds + L_CTL);
    const int tt = 16 * (b & 7) + ((b >> 3) & 15), hf = b >> 7;
    const int ab = b - 160;
    const int f3 = 7 * (b & 7) - ((b & 7) == 7) + (ab >> 3);
    const int g4 = 5 * (b & 7) + ((ab - 54) >> 3);

    const int w = wave - 5, ct = tid - 320;
    const int col = lane & 15, q4 = lane >> 4;
    const bool isP3 = ab >= 0 && ab < 54, isP4 = ab >= 54 && ab < 94;
    const int t0 = 16 * tt + 8 * (lane >> 5), ch0 = 320 * hf + 64 * wave + 2 * (lane & 31);
    u32 kw[11], vw[8], cpf = 0;
    v4u sw;
    if (wave < 5) {
        if (wave == 4 && lane < CODE_LINES) cpf = *(const GAS u32*)(pc0 + umin(((b >> 3) * CODE_LINES + lane) * 64, CODE_MAX));
        CFENCE();
#pragma unroll
        for (int i = 0; i < 11; ++i) { int t = t0 - 3 + i; kw[i] = *(const u32*)(p.k + (t < 0 ? 0 : t) * D_ + ch0); }
#pragma unroll
        for (int i = 0; i < 8; ++i) vw[i] = *(const u32*)(p.v + (t0 + i) * D_ + ch0);
        sw = *(const v4u*)(p.sh + ch0 * 4);
    } else {
        if (ct < 16) ctl[ct] = 0;
    }
    bar();
    u64 wsa = (u64)p.ws;
    asm volatile("" : "+s"(wsa));
    u64 e0 = *(const __attribute__((address_space(4))) u64*)wsa;
    CFENCE();
    u32 tmo;
    u64 epoch;
#define EPOCH() do { asm volatile("" : "+s"(e0) :: "memory"); tmo = now() + TMO; epoch = e0 * 0xD1342543DE82EF95ull + 0x9E3779B97F4A7C15ull; } while (0)
    auto tlast = [&]() __attribute__((always_inline)) {
        u32 t;
        while ((t = __hip_atomic_load(ctl + 15, WG)) == 0) {}
        return (u32)__builtin_amdgcn_readfirstlane(t);
    };
    u32 g1, g2;
    auto top2 = [&]() __attribute__((always_inline)) {
        v2u kp[4];
        const u32 off = W_R + (b & 7) * RS + lane * RSL;
        v2u kq[4];
#pragma unroll
        for (int j = 0; j < 4; ++j) kp[j] = __builtin_amdgcn_raw_buffer_load_b64(wr, off, j * 64 * RSL, SC1);
        for (;;) {
            __builtin_amdgcn_s_sleep(STG_R);
            CFENCE();
#pragma unroll
            for (int j = 0; j < 4; ++j) kq[j] = __builtin_amdgcn_raw_buffer_load_b64(wr, off, j * 64 * RSL, SC1);
            CFENCE();
            if (__all(umax(umax(umax(kp[0].x, kp[0].y), umax(kp[1].x, kp[1].y)), umax(umax(kp[2].x, kp[2].y), umax(kp[3].x, kp[3].y))) != POISON) || past(tmo)) break;
#pragma unroll
            for (int j = 0; j < 4; ++j) kp[j] = __builtin_amdgcn_raw_buffer_load_b64(wr, off, j * 64 * RSL, SC1);
            CFENCE();
            if (__all(umax(umax(umax(kq[0].x, kq[0].y), umax(kq[1].x, kq[1].y)), umax(umax(kq[2].x, kq[2].y), umax(kq[3].x, kq[3].y))) != POISON)) {
#pragma unroll
                for (int j = 0; j < 4; ++j) kp[j] = kq[j];
                break;
            }
        }
        STAMP(8);
        g1 = kp[0].x; g2 = kp[0].y;
        top2_merge(g1, g2, kp[1].x, kp[1].y);
        u32 h1 = kp[2].x, h2 = kp[2].y;
        top2_merge(h1, h2, kp[3].x, kp[3].y);
        top2_merge(g1, g2, h1, h2);
        top2_merge(g1, g2, dppu<DPP_X1>(g1), dppu<DPP_X1>(g2));
        top2_merge(g1, g2, dppu<DPP_X2>(g1), dppu<DPP_X2>(g2));
        top2_merge(g1, g2, dppu<DPP_HM>(g1), dppu<DPP_HM>(g2));
        top2_merge(g1, g2, dppu<DPP_M>(g1), dppu<DPP_M>(g2));
        { auto s = __builtin_amdgcn_permlane16_swap(g1, g1, false, false); auto t = __builtin_amdgcn_permlane16_swap(g2, g2, false, false);
          g1 = s[0]; g2 = t[0]; top2_merge(g1, g2, s[1], t[1]); }
        { auto s = __builtin_amdgcn_permlane32_swap(g1, g1, false, false); auto t = __builtin_amdgcn_permlane32_swap(g2, g2, false, false);
          g1 = s[0]; g2 = t[0]; top2_merge(g1, g2, s[1], t[1]); }
    };
    if (wave < 5) {
        EPOCH();
        {
            if (t0 == 0) { kw[0] = 0; kw[1] = 0; kw[2] = 0; }
            const f2 s0 = {bflo(sw.x), bflo(sw.z)}, s1 = {bfhi(sw.x), bfhi(sw.z)}, s2 = {bflo(sw.y), bflo(sw.w)}, s3 = {bfhi(sw.y), bfhi(sw.w)};
            f2 kf[11], u[8];
#pragma unroll
            for (int i = 0; i < 11; ++i) kf[i] = bfup(kw[i]);
#pragma unroll
            for (int i = 0; i < 8; ++i) u[i] = (kf[i] * s3 + kf[i + 1] * s2 + kf[i + 2] * s1 + kf[i + 3] * s0) * bfup(vw[i]);
            v4u c0 = {san(pk(u[7].x, u[6].x)), san(pk(u[5].x, u[4].x)), san(pk(u[3].x, u[2].x)), san(pk(u[1].x, u[0].x))};
            v4u c1 = {san(pk(u[7].y, u[6].y)), san(pk(u[5].y, u[4].y)), san(pk(u[3].y, u[2].y)), san(pk(u[1].y, u[0].y))};
#pragma unroll
            for (int i = 0; i < 4; ++i) { auto t = __builtin_amdgcn_permlane16_swap(c0[i], c1[i], false, false); c0[i] = t[0]; c1[i] = t[1]; }
            const u32 off = W_KVT + (80 * hf + 16 * wave + ((lane & 15) >> 1)) * 16384 + (255 - (t0 >> 3)) * 64 + (2 * (lane & 1) + ((lane >> 4) & 1)) * 16;
            asm volatile("" : "+v"(c0), "+v"(c1) : "s"(epoch), "v"(off), "v"(cpf));
            STAMP(1);
            while (__hip_atomic_load(ctl + 3, __ATOMIC_RELAXED, __HIP_MEMORY_SCOPE_WORKGROUP) != 4) {}
            CFENCE();
            __builtin_amdgcn_raw_buffer_store_b128(c0, wr, off, 0, SC1);
            __builtin_amdgcn_raw_buffer_store_b128(c1, wr, off + 8 * 16384, 0, SC1);
        }
        if (b >= 160 && wave < 4 && lane == 0) __hip_atomic_fetch_add(ctl + 11, 1u, __ATOMIC_RELAXED, __HIP_MEMORY_SCOPE_WORKGROUP);
        STAMP(2);
        const int jm = ab - 36;
        v4u ps = {POISON, POISON, POISON, POISON};
        v2u ep = {(u32)epoch, (u32)(epoch >> 32)};
        u32 o0 = W_Y + b * YS + lane * 16, o1 = W_END, o2 = W_END, o3 = W_END, o4 = W_FLAGP + (b & 7) * 4096 + lane * 16, o5 = (b & 7) * MS + lane * 16, oh = W_END;
        if (lane < 32) {}
        else if (lane < 48) { if (ab >= 0 && ab < 54) o1 = W_A + ((lane - 32) >> 3) * AS + f3 * 128 + (lane & 7) * 16; }
        if (lane < 8) { o2 = W_R + lane * RS + b * RSL; o3 = W_FLAGP + lane * 4096 + b * 16; }
        if (b < 96 && tid == 0) oh = W_HINT + b * 128;
        const int tok = lane & 7, q0 = 40 * wave + (lane >> 3);
        u32 ox = ((8 * b + tok) * D_ + 4 * q0) * 2;
        LAS u32* pc7 = ctl + 7;
        LAS char* lm = lds + L_MOD + lane * 16;
        u32 wv[5];
        if (wave == 4) {
#pragma unroll
            for (int i = 0; i < 5; ++i) wv[i] = *(const u32*)(p.wr + 128 * i + 2 * lane);
        }
        asm volatile("" : "+v"(ps), "+v"(ep), "+v"(o0), "+v"(o1), "+v"(o2), "+v"(o3), "+v"(o4), "+v"(o5), "+v"(oh), "+v"(ox), "+v"(lm), "+v"(pc7));
        if (b >= 160 && tid == 256) ctl[7] = now();
        constexpr int PER3 = (2560 + 320 * NLATE > 3840 + 256 * NLATE ? 2560 + 320 * NLATE : 3840 + 256 * NLATE) / 20, NP3 = (PER3 + 63) / 64, NPF = NP3 + 5;
        u64 pfa[NPF];
        auto pfd = [&](int id) __attribute__((always_inline)) {
            const int i = umin(id, 1728 * 3 - 1), r = i / 3;
            return (u64)(p.wd + r * D_ + (80 * (b & 7) & ~31) + 32 * (i - r * 3));
        };
        if (wave == 4 && b < 160) {
            const int xc = b & 7, fb = 7 * xc - (xc == 7), nn = xc < 4 ? 5 : 4, nm = (xc < 6 ? 7 : 6) - nn;
#pragma unroll
            for (int u = 0; u < NP3; ++u) {
                const int id = (b >> 3) * PER3 + umin(64 * u + lane, PER3 - 1);
                int r, g;
                if (id < 1280 * nm) { r = nm == 2 ? id >> 1 : id / 3; g = nn + id - r * nm; }
                else {
                    const int i = umin(id - 1280 * nm, NLATE * 64 * nn - 1), h = nn == 4 ? i >> 2 : i / 5, k = h / (8 * NLATE);
                    g = i - h * nn; r = 160 * k + 160 - 8 * NLATE + h - k * (8 * NLATE);
                }
                pfa[u] = (u64)((r < 640 ? p.wg + r * F_ : p.wu + (r - 640) * F_) + 32 * (fb + g));
            }
#pragma unroll
            for (int u = 0; u < 5; ++u) pfa[NP3 + u] = pfd((b >> 3) * 260 + umin(64 * u + lane, 259));
#pragma unroll
            for (int u = 0; u < NPF; ++u) asm volatile("" : "+v"(pfa[u]));
        }
        if (b < 160) {
            if (wave < 4) {
                const u32 tk = now() + DLY_A;
                const int fk = (((b / 80) * 4 + (b % 80) / 20) * 128 + 96 - 32 * wave + (lane & 31)) * 2;
                wait_until(tk - DLY_K);
                while (!__all(__hip_atomic_load(wsq + W_FLAGK / 8 + fk, RLX) == epoch) && !past(tmo)) __builtin_amdgcn_s_sleep(2);
                wait_until(tk);
                STAMP(13);
                v4u r[4];
#pragma unroll
                for (int j = 0; j < 4; ++j) r[j] = (v4u){POISON, POISON, POISON, POISON};
                do {
                    CFENCE();
#pragma unroll
                    for (int j = 0; j < 4; ++j)
                        if (!clean4(r[j])) r[j] = __builtin_amdgcn_raw_buffer_load_b128(wr, W_KVT + b * 16384 + wave * 4096 + lane * 16, j * 1024, SC1);
                } while (!__all(clean4(r[0]) && clean4(r[1]) && clean4(r[2]) && clean4(r[3])) && !past(tmo));
                STAMP(9);
                LAS char* dst = lds + (lane & 3) * CHS + L_R + ((96 * lane) & 127) + (64 * wave + (lane >> 2)) * 16;
#pragma unroll
                for (int j = 0; j < 4; ++j) *(LAS v4u*)(dst + j * 256) = r[j];
                if (wave == 0) __builtin_amdgcn_raw_buffer_store_b128((v4u){ep.x, ep.y, now(), 0u}, wr, oh, 0, SC1);
                if (lane == 0) __hip_atomic_fetch_max(ctl + 7, now(), WG);
            }
            bar();
        } else if (DLY_T) wait_until(now() + DLY_T);
        const u32 tkv = now();
        LAS u32* const c7 = pc7;
        STAMP(11);
        auto modok = [&]() __attribute__((always_inline)) {
            while (__hip_atomic_load(ctl + 12, __ATOMIC_RELAXED, __HIP_MEMORY_SCOPE_WORKGROUP) == 0) {}
            CFENCE();
        };
        if (wave == 4) {
            if (b >= 160 && DLY_QA) wait_until(tkv + DLY_QA);
            const int nt = b < 160 || !PSHIFT ? 1 : jm >= 0 ? 0 : ab < 24 ? 3 : 2;
#pragma unroll 1
            for (int n = 0; n < nt; ++n) {
#pragma unroll
                for (int i = 0; i < 15; ++i) __builtin_amdgcn_raw_buffer_store_b128(ps, wr, o0, n * (36 * YS) + i * 1024, SC1);
            }
            if (jm >= 0) {
                __builtin_amdgcn_raw_buffer_store_b128(ps, wr, W_MOD + (lane >> 3) * MS + jm * 128 + (lane & 7) * 16, 0, SC1);
                if (jm >= 40 && jm < 50) {
                    __builtin_amdgcn_raw_buffer_store_b128(ps, wr, W_MOD + (lane >> 3) * MS + 7680 + (jm - 40) * 256 + (lane & 7) * 16, 0, SC1);
                    __builtin_amdgcn_raw_buffer_store_b128(ps, wr, W_MOD + (lane >> 3) * MS + 7680 + (jm - 40) * 256 + (lane & 7) * 16, 128, SC1);
                }
            }
            __builtin_amdgcn_raw_buffer_store_b128(ps, wr, o1, 0, SC1);
            __builtin_amdgcn_raw_buffer_store_b64((v2u){ps.x, ps.y}, wr, o2, 0, SC1);
            __builtin_amdgcn_raw_buffer_store_b32(*(volatile LAS u32*)c7, wr, o3, 8, SC1);
            if (b >= 160 && lane == 0) __hip_atomic_fetch_add(ctl + 11, 1u, WG);
            DRAIN();
            __builtin_amdgcn_raw_buffer_store_b64(ep, wr, o3, 0, SC1);
            STAMP(3);
            u32 tb = tkv, d[NPF];
            if (b >= 160) {
                ctl[14] = 1;
                v4u h;
                for (;;) {
                    h = __builtin_amdgcn_raw_buffer_load_b128(wr, W_HINT + ab * 128, 0, SC1);
                    if ((h.x == ep.x && h.y == ep.y) || past(tmo)) break;
                    __builtin_amdgcn_s_sleep(4);
                    CFENCE();
                }
                const u32 t = now(), age = t - (u32)__builtin_amdgcn_readfirstlane(h.z);
                tb = t - umin(age, 400u) + DLY_AX;
                ctl[6] = tb | 1u;
            }
            auto prefetch = [&]() __attribute__((always_inline)) {
                if (b < 160) {
                    wait_until(tkv + DLY_F);
                    CFENCE();
#pragma unroll
                    for (int u = 0; u < NPF; ++u) d[u] = *(const GAS u32*)pfa[u];
                    CFENCE();
                }
            };
            if (PF_POS == 0) prefetch();
            v4u fp[4];
            v4u m[10];
            wait_until(tb + DLY_T1);
            CFENCE();
#pragma unroll
            for (int j = 0; j < 4; ++j) fp[j] = __builtin_amdgcn_raw_buffer_load_b128(wr, o4, j * 1024, SC1);
            DRAIN();
            asm volatile("" : "+v"(fp[0]), "+v"(fp[1]), "+v"(fp[2]), "+v"(fp[3]));
            STAMPX(2);
            if (PF_POS == 1) prefetch();
            auto allp = [&]() __attribute__((always_inline)) {
                return __all(fp[0].x == ep.x && fp[0].y == ep.y && fp[1].x == ep.x && fp[1].y == ep.y && fp[2].x == ep.x && fp[2].y == ep.y && fp[3].x == ep.x && fp[3].y == ep.y);
            };
            while (!allp() && !past(tmo)) {
                CFENCE();
#pragma unroll
                for (int j = 0; j < 4; ++j) fp[j] = __builtin_amdgcn_raw_buffer_load_b128(wr, o4, j * 1024, SC1);
            }
            if (b == 0 && lane == 0) __hip_atomic_store(wsq + W_EPOCH / 8, epoch, RLX);
            STAMP(12);
            if (b >= 160) wait_until(tb + DLY_H);
            CFENCE();
#pragma unroll
            for (int i = 0; i < 10; ++i) m[i] = __builtin_amdgcn_raw_buffer_load_b128(wr, o5, W_MOD + i * 1024, SC1);
            bar();
            u32 tl;
            {
                u32 dmx = umax(umax(fp[0].z - tb + 1000u, fp[1].z - tb + 1000u), umax(fp[2].z - tb + 1000u, fp[3].z - tb + 1000u));
                dmx = umax(dmx, dppu<DPP_X1>(dmx)); dmx = umax(dmx, dppu<DPP_X2>(dmx)); dmx = umax(dmx, dppu<DPP_HM>(dmx)); dmx = umax(dmx, dppu<DPP_M>(dmx));
                const u32 d4 = umax(umax(__builtin_amdgcn_readlane(dmx, 0), __builtin_amdgcn_readlane(dmx, 16)), umax(__builtin_amdgcn_readlane(dmx, 32), __builtin_amdgcn_readlane(dmx, 48)));
                tl = (tb - 1000u + umin(d4, 1200u)) | 1u;
                ctl[15] = tl;
            }
            if (PF_POS < 2 && b < 160) {
#pragma unroll
                for (int u = 0; u < NPF; ++u) asm volatile("" :: "v"(d[u]));
            }
            for (;;) {
                u32 mx = 0;
#pragma unroll
                for (int i = 0; i < 10; ++i) { *(LAS v4u*)(lm + i * 1024) = m[i]; mx = umax(umax(mx, m[i].x), umax(umax(m[i].y, m[i].z), m[i].w)); }
                STAMPX(13);
                if (__all(mx != POISON) || past(tmo)) break;
                CFENCE();
#pragma unroll
                for (int i = 0; i < 10; ++i)
                    if (!__all(clean4(m[i]))) m[i] = __builtin_amdgcn_raw_buffer_load_b128(wr, o5, W_MOD + i * 1024, SC1);
            }
            CFENCE();
            ctl[12] = 1;
            STAMPX(1);
            float c1 = 0.f, c2 = 0.f;
#pragma unroll
            for (int i = 0; i < 5; ++i) {
                const f2 cwv = *(const LAS f2*)(lds + L_MOD + 7680 + (128 * i + 2 * lane) * 4), sbv = bfup(*(const LAS u32*)(lds + L_MOD + 3 * 1280 + (128 * i + 2 * lane) * 2)) * bfup(wv[i]);
                c1 += cwv.x + cwv.y; c2 += sbv.x + sbv.y;
            }
            float q = add32(c1, c2);
            q = add16(q, q);
            q += dppf<DPP_ROR8>(q); q += dppf<DPP_ROR4>(q); q += dppf<DPP_X2>(q); q += dppf<DPP_X1>(q);
            if ((lane & 31) == 0) *(LAS float*)(lds + L_CC + (lane >> 5) * 4) = q;
            bar();
            if (ab >= 54 && ab < 94) {
                wait_until(now() + DLY_RB); wait_until(tl + DLY_R4);
                top2();
                if (lane == 0) ctl[10] = now() | 1u;
                const int d = 16 * g4 + 2 * (lane & 7), hi = (lane >> 3) & 1;
                const int t1 = 2047 - (g1 & 0x7FF), t2 = 2047 - (g2 & 0x7FF), tk = (hi ? t2 : t1) >> 3;
                const u32 sl = hi && (t1 >> 3) == (t2 >> 3) ? 1280 : 0;
                u32 xw, hw;
                do {
                    CFENCE();
                    xw = __builtin_amdgcn_raw_buffer_load_b32(wr, W_Y + 12800 + tk * YS + sl + d * 2, 0, SC1); hw = __builtin_amdgcn_raw_buffer_load_b32(wr, W_Y + 10240 + tk * YS + sl + d * 2, 0, SC1);
                } while (!__all(umax(xw, hw) != POISON) && !past(tmo));
                u32 u = ((hi ? g2 : g1) & ~0x7FFu) | 0x400u;
                u = (u & 0x80000000u) ? (u & 0x7FFFFFFFu) : ~u;
                const float sg = rnd1(__builtin_amdgcn_rcpf(1.f + __expf(-rnd1(__uint_as_float(u)))));
                if (lane < 16) { const v4u e = {xw, hw, __float_as_uint(sg), (u32)((hi ? t2 : t1) * D_ + d)}; *(LAS v4u*)(lds + L_RED + 1024 + lane * 16) = e; }
                wait_until(tl + DLY_XF);
                while (!__all(__hip_atomic_load(wsq + (W_XF + tk * 64 + (d / 160) * 8) / 8, RLX) == epoch) && !past(tmo)) __builtin_amdgcn_s_sleep(2);
                CFENCE();
                ctl[9] = 1;
                STAMPX(2);
            }
            return;
        }
        v2u xw[5];
        wait_until(tkv + (b < 160 ? DLY_XC : DLY_XL));
        CFENCE();
#pragma unroll
        for (int i = 0; i < 5; ++i) xw[i] = *(const v2u*)((const char*)p.x + ox + 64 * i);
        f2 xf[10];
        auto ln1 = [&]() __attribute__((always_inline)) {
#pragma unroll
            for (int i = 0; i < 5; ++i) { xf[2 * i] = bfup(xw[i].x); xf[2 * i + 1] = bfup(xw[i].y); }
            f2 a = xf[0] + xf[1], q = xf[0] * xf[0], q2 = xf[1] * xf[1];
#pragma unroll
            for (int i = 2; i < 10; i += 2) { a += xf[i] + xf[i + 1]; q += xf[i] * xf[i]; q2 += xf[i + 1] * xf[i + 1]; }
            q += q2;
            float v = add32(a.x + a.y, q.x + q.y);
            v = add16(v, v);
            v += dppf<DPP_ROR8>(v);
            if ((lane & 24) == 0) *(LAS float*)(lds + L_LN1 + tok * 64 + (lane >> 5) * 16 + wave * 4) = v;
        };
        if (b >= 160) ln1();
        STAMP(4);
        bar();
        if (b < 160) {
            ln1();
            asm volatile("s_waitcnt lgkmcnt(0)" ::: "memory");
            if (lane == 0) __hip_atomic_fetch_add(ctl + 13, 1u, __ATOMIC_RELAXED, __HIP_MEMORY_SCOPE_WORKGROUP);
        }
        const u32 tl = tlast(), tY2 = tl + DLY_Y, tY = DLY_YE ? tY2 - DLY_YE : tY2;
        v2u yw[5];
        const u32 yoff = W_Y + b * YS + wave * 2560 + lane * 8;
        while (__hip_atomic_load(ctl + 12, __ATOMIC_RELAXED, __HIP_MEMORY_SCOPE_WORKGROUP) == 0 && !past(tY)) {}
        auto yok = [&]() __attribute__((always_inline)) {
            return __all(umax(umax(umax(yw[0].x, yw[0].y), umax(yw[1].x, yw[1].y)), umax(umax(umax(yw[2].x, yw[2].y), umax(yw[3].x, yw[3].y)), umax(yw[4].x, yw[4].y))) != POISON);
        };
        auto yretry = [&]() __attribute__((always_inline)) {
            CFENCE();
#pragma unroll
            for (int k = 0; k < 5; ++k)
                if (umax(yw[k].x, yw[k].y) == POISON) yw[k] = __builtin_amdgcn_raw_buffer_load_b64(wr, yoff, k * 512, SC1);
        };
        const bool yfirst = past(tY);
        if (yfirst) {
            STAMP(6);
#pragma unroll
            for (int k = 0; k < 5; ++k) yw[k] = __builtin_amdgcn_raw_buffer_load_b64(wr, yoff, k * 512, SC1);
            while (__hip_atomic_load(ctl + 12, __ATOMIC_RELAXED, __HIP_MEMORY_SCOPE_WORKGROUP) == 0 && !yok()) if (past(tY2)) yretry();
        }
        modok();
        if (b < 160) { while (__hip_atomic_load(ctl + 13, __ATOMIC_RELAXED, __HIP_MEMORY_SCOPE_WORKGROUP) != 4) {} CFENCE(); }
        f2 gh[10], cw[10];
        {
            const f4 a = *(const LAS f4*)(lds + L_LN1 + tok * 64), c = *(const LAS f4*)(lds + L_LN1 + tok * 64 + 16);
            float m = ((a.x + a.y) + (a.z + a.w)) * (1.f / D_);
            const float rs = __builtin_amdgcn_rcpf(rnd1(__builtin_amdgcn_sqrtf(rnd1(rnd1(fmaxf(((c.x + c.y) + (c.z + c.w)) * (1.f / D_) - m * m, 0.f)) + 1e-5f))));
            m = rnd1(m);
            const LAS char* mb = lds + L_MOD + q0 * 8;
#pragma unroll
            for (int i = 0; i < 5; ++i) {
                const v2u sa = *(const LAS v2u*)(mb + i * 64), ca = *(const LAS v2u*)(mb + 1280 + i * 64), ga = *(const LAS v2u*)(mb + 2560 + i * 64);
                const f4 w4 = *(const LAS f4*)(lds + L_MOD + 7680 + (q0 + 8 * i) * 16);
                cw[2 * i] = (f2){w4.x, w4.y}; cw[2 * i + 1] = (f2){w4.z, w4.w};
                gh[2 * i] = bfup(ga.x) * ((xf[2 * i] - m) * rs * bfup(ca.x) + bfup(sa.x));
                gh[2 * i + 1] = bfup(ga.y) * ((xf[2 * i + 1] - m) * rs * bfup(ca.y) + bfup(sa.y));
            }
        }
        STAMP(5);
        if (!yfirst) {
            wait_until(tY);
            STAMP(6);
            CFENCE();
#pragma unroll
            for (int k = 0; k < 5; ++k) yw[k] = __builtin_amdgcn_raw_buffer_load_b64(wr, yoff, k * 512, SC1);
        }
        asm volatile("" : "+v"(gh[0]), "+v"(gh[1]), "+v"(gh[2]), "+v"(gh[3]), "+v"(gh[4]), "+v"(gh[5]), "+v"(gh[6]), "+v"(gh[7]), "+v"(gh[8]), "+v"(gh[9]));
        if (DLY_YE && !yok()) wait_until(tY2);
        while (!yok() && !past(tmo)) yretry();
        STAMP(7);
        f2 x1[10];
#pragma unroll
        for (int i = 0; i < 5; ++i) { x1[2 * i] = gh[2 * i] * bfup(yw[i].x) + xf[2 * i]; x1[2 * i + 1] = gh[2 * i + 1] * bfup(yw[i].y) + xf[2 * i + 1]; }
        {
            f2 a = x1[0], a2 = x1[1], q = x1[0] * x1[0], q2 = x1[1] * x1[1], r = x1[0] * cw[0], r2 = x1[1] * cw[1];
#pragma unroll
            for (int i = 2; i < 10; i += 2) { a += x1[i]; a2 += x1[i + 1]; q += x1[i] * x1[i]; q2 += x1[i + 1] * x1[i + 1]; r += x1[i] * cw[i]; r2 += x1[i + 1] * cw[i + 1]; }
            a += a2; q += q2; r += r2;
            const float s3 = r.x + r.y;
            float v = add16(add32(a.x + a.y, q.x + q.y), add32(s3, s3));
            v += dppf<DPP_ROR8>(v);
            if ((lane & 8) == 0 && lane < 48) *(LAS float*)(lds + L_P2 + tok * 64 + (lane >> 4) * 16 + wave * 4) = v;
        }
        bar();
        u32 k1, k2;
        float m, rs;
        {
            const LAS f4* q = (const LAS f4*)(lds + L_P2 + tok * 64);
            const f4 a = q[0], c = q[1], d = q[2];
            const f2 cc = *(const LAS f2*)(lds + L_CC);
            m = ((a.x + a.y) + (a.z + a.w)) * (1.f / D_);
            rs = __builtin_amdgcn_rcpf(rnd1(__builtin_amdgcn_sqrtf(rnd1(rnd1(fmaxf(((d.x + d.y) + (d.z + d.w)) * (1.f / D_) - m * m, 0.f)) + 1e-5f))));
            m = rnd1(m);
            const float r = rs * (((c.x + c.y) + (c.z + c.w)) - m * cc.x) + cc.y;
            u32 u = __float_as_uint(r);
            u = (u & 0x80000000u) ? ~u : (u | 0x80000000u);
            const u32 key = san((u & ~0x7FFu) | (u32)(2047 - (8 * b + tok))), key2 = dppu<DPP_X1>(key);
            k1 = umax(key, key2); k2 = umin(key, key2);
            top2_merge(k1, k2, dppu<DPP_X2>(k1), dppu<DPP_X2>(k2));
            top2_merge(k1, k2, dppu<DPP_HM>(k1), dppu<DPP_HM>(k2));
            if (tid < 8) { const v2u kk = {k1, k2}; __builtin_amdgcn_raw_buffer_store_b64(kk, wr, W_R + tid * RS + b * RSL, 0, SC1); }
        }
        STAMP(8);
        {
            LAS char* sw = lds + L_STG + wave * (3 * STA) + tok * STR + (lane >> 3) * 8;
            const LAS char* mb = lds + L_MOD + q0 * 8;
            v2u hw[5];
#pragma unroll
            for (int i = 0; i < 5; ++i) {
                const v2u cb = *(const LAS v2u*)(mb + 4 * 1280 + i * 64), sb = *(const LAS v2u*)(mb + 3 * 1280 + i * 64);
                hw[i].x = san(pk2((x1[2 * i] - m) * rs * bfup(cb.x) + bfup(sb.x)));
                hw[i].y = san(pk2((x1[2 * i + 1] - m) * rs * bfup(cb.y) + bfup(sb.y)));
                *(LAS v2u*)(sw + i * 64) = hw[i];
            }
            const int sl = lane >= 20, pc = lane - 20 * sl, r = (2047 - ((sl ? k2 : k1) & 0x7FF)) & 7;
            const LAS char* cr = lds + L_STG + wave * (3 * STA) + r * STR + pc * 16;
            if (lane < 40) __builtin_amdgcn_raw_buffer_store_b128(*(const LAS v4u*)cr, wr, W_Y + b * YS + 10240 + sl * 1280 + wave * 320 + pc * 16, 0, SC1);
#pragma unroll
            for (int i = 0; i < 5; ++i) {
                const v2u gb = *(const LAS v2u*)(mb + 5 * 1280 + i * 64);
                const v2u o = {pk2(bfup(gb.x) * bfup(hw[i].x) + x1[2 * i]), pk2(bfup(gb.y) * bfup(hw[i].y) + x1[2 * i + 1])}, x = {san(pk2(x1[2 * i])), san(pk2(x1[2 * i + 1]))};
                *(LAS v2u*)(sw + STA + i * 64) = o;
                *(LAS v2u*)(sw + 2 * STA + i * 64) = x;
            }
            v4u ov[3], xv;
#pragma unroll
            for (int i = 0; i < 3; ++i) { const int e = umin(64 * i + lane, 159), rr = e / 20; ov[i] = *(const LAS v4u*)(lds + L_STG + wave * (3 * STA) + STA + rr * STR + (e - 20 * rr) * 16); }
            xv = *(const LAS v4u*)(cr + 2 * STA);
            if (lane < 40) __builtin_amdgcn_raw_buffer_store_b128(xv, wr, W_Y + b * YS + 12800 + sl * 1280 + wave * 320 + pc * 16, 0, SC1);
            wait_until(tl + (ab >= 0 && ab < 54 ? DLY_O3 : DLY_O + ((b >> 3) & 15) * STG_O));
            const __amdgpu_buffer_rsrc_t orr = __builtin_amdgcn_make_buffer_rsrc(p.out, 0, S_ * D_ * 2, 0x00020000);
#pragma unroll
            for (int i = 0; i < 3; ++i) {
                const int e = 64 * i + lane, rr = e / 20;
                if (e < 160) __builtin_amdgcn_raw_buffer_store_b128(ov[i], orr, (8 * b + rr) * 1280 + wave * 320 + (e - 20 * rr) * 16, 0, SC1);
            }
            DRAIN();
            if (lane == 0) __hip_atomic_store(wsq + (W_XF + b * 64 + wave * 8) / 8, epoch, RLX);
        }
        STAMP(10);
        return;
    }

    v4u c1[4];
    u32 db;
    {
        const v4u ps = {POISON, POISON, POISON, POISON};
        const u32 off = W_KVT + (80 * hf + 20 * w + (lane >> 3)) * 16384 + (254 - 2 * tt) * 64 + (lane & 7) * 16;
        __builtin_amdgcn_raw_buffer_store_b128(ps, wr, off, 0, SC1);
        __builtin_amdgcn_raw_buffer_store_b128(ps, wr, off + 8 * 16384, 0, SC1);
        __builtin_amdgcn_raw_buffer_store_b128(ps, wr, lane < 32 ? off + 16 * 16384 : W_END, 0, SC1);
        CFENCE();
        const __amdgpu_buffer_rsrc_t kr = __builtin_amdgcn_make_buffer_rsrc((void*)p.sk, 0, D_ * S_ * 2, 0x00020000), dr = __builtin_amdgcn_make_buffer_rsrc((void*)p.sD, 0, D_ * 2, 0x00020000);
#pragma unroll
        for (int j = 0; j < 4; ++j) c1[j] = __builtin_amdgcn_raw_buffer_load_b128(kr, (4 * b + w) * (S_ * 2) + lane * 16 + j * 1024, 0, 0);
        db = __builtin_amdgcn_raw_buffer_load_b16(dr, (4 * b + w) * 2 + (lane >> 8), 0, 0);
        EPOCH();
    }
    auto flagK = [&](int younger) __attribute__((always_inline)) {
        if (younger == 5) asm volatile("s_waitcnt vmcnt(5)" ::: "memory"); else asm volatile("s_waitcnt vmcnt(%0)" :: "n"(5 + ME) : "memory");
        asm volatile("" :: "v"(cpf));
        if (lane == 0) {
            __hip_atomic_fetch_add(ctl + 3, 1u, __ATOMIC_RELAXED, __HIP_MEMORY_SCOPE_WORKGROUP);
            __hip_atomic_store(wsq + W_FLAGK / 8 + ((hf * 4 + w) * 128 + tt) * 2, epoch, RLX);
        }
    };
    auto last = [&]() __attribute__((always_inline)) {
        asm volatile("s_waitcnt lgkmcnt(0)" ::: "memory");
        u32 old = 0;
        if (lane == 0) old = __hip_atomic_fetch_add(ctl + 2, 1u, __ATOMIC_RELAXED, __HIP_MEMORY_SCOPE_WORKGROUP);
        const bool l = __builtin_amdgcn_readfirstlane(old) == 3;
        CFENCE();
        return l;
    };
#define WINNERS() const int t1 = 2047 - (g1 & 0x7FF), t2 = 2047 - (g2 & 0x7FF); \
    const u32 sl2 = (t1 >> 3) == (t2 >> 3) ? 1280 : 0; \
    const u32 row1 = W_Y + 10240 + (t1 >> 3) * YS, row2 = W_Y + 10240 + (t2 >> 3) * YS + sl2; \
    const v4u z4 = {0, 0, 0, 0};
    if (b < 160) {
        LAS char* cbase = lds + w * CHS;
        flagK(5);
        STAMP(1);
        LAS char* rbase = cbase + L_R + ((96 * w) & 127);
        {
            const v4u z4 = {0, 0, 0, 0};
            if (lane < 30) *(LAS v4u*)(rbase + 4096 + lane * 16) = z4;
            if (lane == 0) c1[0].x = (c1[0].x & 0xFFFF0000u) | (pk(bflo(c1[0].x) + bflo(db), 0.f) & 0xFFFFu);
            const u32 k0 = __builtin_amdgcn_readfirstlane(c1[0].x), k1 = __builtin_amdgcn_readfirstlane(c1[0].y);
            if (lane < 8) {
                v4u h = z4;
                if (lane == 4) h.w = k0 << 16;
                if (lane == 5) h.w = k0;
                if (lane == 6) { h.z = k0 << 16; h.w = (k0 >> 16) | (k1 << 16); }
                if (lane == 7) { h.z = k0; h.w = k1; }
                *(LAS v4u*)(cbase + (lane & 3) * KCS + (lane >> 2) * 16) = h;
            }
#pragma unroll
            for (int j = 0; j < 4; ++j) {
                const u32 n0 = j < 3 ? __builtin_amdgcn_readfirstlane(c1[j < 3 ? j + 1 : j].x) : 0u, n1 = j < 3 ? __builtin_amdgcn_readfirstlane(c1[j < 3 ? j + 1 : j].y) : 0u;
                const u32 e[6] = {c1[j].x, c1[j].y, c1[j].z, c1[j].w,
                                  (u32)__builtin_amdgcn_update_dpp((int)n0, (int)c1[j].x, 0x130, 0xf, 0xf, false), (u32)__builtin_amdgcn_update_dpp((int)n1, (int)c1[j].y, 0x130, 0xf, 0xf, false)};
                u32 o[5];
#pragma unroll
                for (int i = 0; i < 5; ++i) o[i] = __builtin_amdgcn_alignbit(e[i + 1], e[i], 16);
                LAS char* dst = cbase + (64 * j + lane + 2) * 16;
#pragma unroll
                for (int m = 0; m < 2; ++m) {
                    const v4u a = {o[m], o[m + 1], o[m + 2], o[m + 3]}, c = {e[m + 1], e[m + 2], e[m + 3], e[m + 4]};
                    *(LAS v4u*)(dst + (2 * m) * KCS) = a;
                    *(LAS v4u*)(dst + (2 * m + 1) * KCS) = c;
                }
            }
        }
        STAMPX(3);
        const LAS char* pa = cbase + (col & 3) * KCS + (2 * q4 + (col >> 2)) * 8;
        const LAS char* pb = rbase + 480 + 16 * q4 - 32 * col;
        const LAS char* pa2 = pa + 8;
        asm volatile("" : "+v"(pa2));
        auto lda = [&](int o) __attribute__((always_inline)) {
            const v2u lo = *(const LAS v2u*)(pa + o), hi = *(const LAS v2u*)(pa2 + o);
            const v4u f = {lo.x, lo.y, hi.x, hi.y};
            return __builtin_bit_cast(bfx8, f);
        };
#define LDA(a, r) lda((a) * 512 + (r) * 64)
#define LDB(bb, r) (*(const LAS bfx8*)(pb + 3584 - (bb) * 512 + (r) * 64))
        f4 acc[8];
#pragma unroll
        for (int i = 0; i < 8; ++i) { acc[i] = (f4){0.f, 0.f, 0.f, 0.f}; asm volatile("" : "+v"(acc[i])); }
        bfx8 A[10], B[10];
#pragma unroll
        for (int j = 0; j < 8; ++j) A[j] = LDA(j, 0);
        asm volatile("" : "+v"(A[0]), "+v"(A[1]), "+v"(A[2]), "+v"(A[3]), "+v"(A[4]), "+v"(A[5]), "+v"(A[6]), "+v"(A[7]));
        asm volatile("" :: "v"(cpf));
        STAMP(11);
        if (lane == 0) __hip_atomic_fetch_max(ctl + 7, now(), WG);
        bar();
        STAMP(4);
        B[0] = LDB(0, 0); B[7] = LDB(7, 0); B[6] = LDB(6, 0); B[1] = LDB(1, 0); B[5] = LDB(5, 0); B[2] = LDB(2, 0); B[3] = LDB(3, 0); B[4] = LDB(4, 0);
        __builtin_amdgcn_sched_barrier(0);
        asm volatile("s_nop 1");
        __builtin_amdgcn_sched_barrier(0);
#define IX(j, r) ((j) < 2 ? (j) + 8 * ((r) & 1) : (j))
#define MM(a, bb) do { asm volatile("v_mfma_f32_16x16x32_bf16 %0, %1, %2, %0" : "+v"(acc[(a) + (bb)]) : "v"(A[IX(a, r)]), "v"(B[IX(bb, r)])); __builtin_amdgcn_sched_barrier(0); } while (0)
#define NA(j) do { if (r < 7) A[IX(j, r + 1)] = LDA(j, r + 1); __builtin_amdgcn_sched_barrier(0); } while (0)
#define NB(j) do { if (r < 7) B[IX(j, r + 1)] = LDB(j, r + 1); __builtin_amdgcn_sched_barrier(0); } while (0)
#pragma unroll
        for (int r = 0; r < 8; ++r) {
            MM(7, 0); NA(0); MM(6, 0); NB(0); MM(0, 7); NA(7); MM(0, 6); NB(7); MM(6, 1); NA(1);
            MM(5, 0); NB(1); MM(1, 6); NA(6); MM(0, 5); NB(6); MM(5, 1); MM(5, 2); MM(1, 5); NA(5); MM(2, 5);
            MM(4, 0); NB(5); MM(4, 1); MM(4, 2); MM(4, 3); MM(0, 4); NA(4); MM(1, 4); MM(2, 4); MM(3, 4);
            MM(3, 0); NB(4); MM(3, 1); MM(3, 2); MM(3, 3); MM(0, 3); NA(3); MM(1, 3); MM(2, 3);
            MM(2, 0); NB(3); MM(2, 1); MM(2, 2); MM(0, 2); NA(2); MM(1, 2);
            MM(1, 0); NB(2); MM(1, 1); MM(0, 1); MM(0, 0);
        }
        asm volatile("s_nop 7\n\ts_nop 1" : "+v"(acc[0]), "+v"(acc[1]), "+v"(acc[2]), "+v"(acc[3]), "+v"(acc[4]), "+v"(acc[5]), "+v"(acc[6]), "+v"(acc[7]));
        __builtin_amdgcn_s_setprio(3);
        STAMP(5);
#pragma unroll
        for (int i = 0; i < 8; ++i) {
            LAS u32* d = (LAS u32*)(cbase + (64 * i + 4 * col + q4) * 4);
            d[0] = pk(acc[i].x, acc[i].z);
            d[512] = pk(acc[i].y, acc[i].w);
        }
        bar();
        {
            const LAS char* tb = lds + (col >> 2) * CHS + (16 * q4 + 4 * (col & 3)) * 2;
#pragma unroll
            for (int it = 0; it < 4; ++it) {
                const int P0 = (256 * w + 64 * it + 64 * b) & 1023;
                const s4 e = __builtin_amdgcn_ds_read_tr16_b64_v4i16((LAS s4*)(tb + P0 * 2));
                const s4 o = __builtin_amdgcn_ds_read_tr16_b64_v4i16((LAS s4*)(tb + P0 * 2 + 2048));
                const v2u e2 = __builtin_bit_cast(v2u, e), o2 = __builtin_bit_cast(v2u, o);
                const v4u y = {san(e2.x), san(e2.y), san(o2.x), san(o2.y)};
                const int P = P0 + lane;
                __builtin_amdgcn_raw_buffer_store_b128(y, wr, W_Y + (P >> 2) * YS + b * 64 + (P & 3) * 16, 0, SC1);
            }
        }
        STAMP(6);
        bar();
    } else {
        const int jm = ab - 36;
        v4u mw[20]; u32 cw[20], wrv = 0;
        const __amdgpu_buffer_rsrc_t mr = __builtin_amdgcn_make_buffer_rsrc((void*)p.wm, 0, 6 * D_ * D_ * 2, 0x00020000);
        const u32 mo = jm >= 0 ? (ct >> 3) * (12 * D_) + 128 * jm + 16 * (ct & 7) : 6 * D_ * D_ * 2;
#pragma unroll
        for (int i = 0; i < ME; ++i) mw[i] = __builtin_amdgcn_raw_buffer_load_b128(mr, mo + i * (32 * 12 * D_), 0, 0);
        flagK(0);
        while (__hip_atomic_load(ctl + 11, __ATOMIC_RELAXED, __HIP_MEMORY_SCOPE_WORKGROUP) < NW_W) __builtin_amdgcn_s_sleep(1);
        if (DLY_W) wait_until(now() + DLY_W);
        if (jm < 0) {
            if (MPF) {
                const int xc = b & 7, j0 = (xc + 4) & 7, ng = j0 < 4 ? 8 : 7, nh = xc < 4 ? 5 : 4, per = (ng * MPF + nh - 1) / nh;
#pragma unroll
                for (int i = 0; i < MPF / 16; ++i) {
                    const int l = umin((ab >> 3) * per + umin(32 * i + (ct >> 3), per - 1), ng * MPF - 1), r = ng == 8 ? l >> 3 : l / 7;
                    mw[i] = *(const v4u*)(p.wm + (640 - MPF + r) * (6 * D_) + 64 * (j0 + 8 * (l - r * ng)) + 8 * (ct & 7));
                }
#pragma unroll
                for (int i = 0; i < MPF / 16; ++i) asm volatile("" :: "v"(mw[i]));
            }
            wait_until(now() + DLY_N);
        }
        if (jm >= 0) {
#pragma unroll
            for (int i = 0; i < 20; ++i) {
                const int row = 32 * i + (ct >> 3);
                if (i >= ME) mw[i] = __builtin_amdgcn_raw_buffer_load_b128(mr, mo + i * (32 * 12 * D_), 0, 0);
                cw[i] = p.c[row];
            }
            wrv = *(const u32*)(p.wr + 64 * (jm % 10) + 2 * (lane & 31));
        }
        if (jm >= 0) {
            f2 a[4] = {{0.f, 0.f}, {0.f, 0.f}, {0.f, 0.f}, {0.f, 0.f}};
#pragma unroll
            for (int i = 0; i < 20; ++i) {
                const float cv = bflo(cw[i]);
                const float s = bflo(pk(cv * __builtin_amdgcn_rcpf(1.f + __expf(-cv)), 0.f));
                a[0] += bfup(mw[i].x) * s; a[1] += bfup(mw[i].y) * s; a[2] += bfup(mw[i].z) * s; a[3] += bfup(mw[i].w) * s;
            }
            float r0 = add16(add32(a[0].x, a[2].x), add32(a[1].x, a[3].x)), r1 = add16(add32(a[0].y, a[2].y), add32(a[1].y, a[3].y));
            r0 += dppf<DPP_ROR8>(r0); r1 += dppf<DPP_ROR8>(r1);
            if ((lane & 8) == 0) { f2 r = {r0, r1}; *(LAS f2*)(lds + L_MODP + w * 256 + (8 * (lane & 7) + 2 * q4) * 4) = r; }
            asm volatile("s_waitcnt lgkmcnt(0)" ::: "memory");
            u32 old = 0;
            if (lane == 0) old = __hip_atomic_fetch_add(ctl + 4, 1u, __ATOMIC_RELAXED, __HIP_MEMORY_SCOPE_WORKGROUP);
            if (__builtin_amdgcn_readfirstlane(old) == 3) {
                CFENCE();
                {
                    const LAS f2* q = (const LAS f2*)(lds + L_MODP) + (lane & 31);
                    f2 v = rnd((q[0] + q[32]) + (q[64] + q[96]));
                    const int j = jm / 10;
                    if (j == 1 || j == 4) v = rnd(v + 1.f);
                    const v2u cv = __builtin_bit_cast(v2u, v * bfup(wrv));
                    while (!__hip_atomic_load(ctl + 14, __ATOMIC_RELAXED, __HIP_MEMORY_SCOPE_WORKGROUP)) {}
                    CFENCE();
#pragma unroll
                    for (int r = 0; r < 4; ++r) __builtin_amdgcn_raw_buffer_store_b32(san(pk2(v)), wr, W_MOD + (lane >> 5) * MS + jm * 128 + (lane & 31) * 4, 2 * r * MS, SC1);
                    if (j == 4) {
#pragma unroll
                        for (int r = 0; r < 4; ++r) __builtin_amdgcn_raw_buffer_store_b64((v2u){san(cv.x), san(cv.y)}, wr, W_MOD + (lane >> 5) * MS + 7680 + (jm - 40) * 256 + (lane & 31) * 8, 2 * r * MS, SC1);
                    }
                }
                CFENCE();
                ctl[5] = 1;
            }
            while (!__hip_atomic_load(ctl + 5, __ATOMIC_RELAXED, __HIP_MEMORY_SCOPE_WORKGROUP)) __builtin_amdgcn_s_sleep(1);
            if (DLY_V) wait_until(now() + DLY_V);
        }
        asm volatile("" :: "v"(cpf));
        STAMP(1);
        auto wl = [&]() __attribute__((always_inline)) {
            u32 t;
            while ((t = __hip_atomic_load(ctl + 6, __ATOMIC_RELAXED, __HIP_MEMORY_SCOPE_WORKGROUP)) == 0) __builtin_amdgcn_s_sleep(2);
            wait_until(__builtin_amdgcn_readfirstlane(t) + DLY_WL);
        };
        if (isP3) {
            bfx8 wf[20];
            if (jm >= 0) { if (WL3) wl(); else { bar(); bar(); } }
#pragma unroll
            for (int i = 0; i < 20; ++i) {
                if (i == 20 - NLATE && jm < 0) { if (WL3) wl(); else { bar(); bar(); } }
                const int row = 160 * w + 8 * i + (lane >> 3), sg = lane & 7;
                const u16* src = (sg < 4 ? p.wg : p.wu) + row * F_ + 32 * f3 + 8 * (sg & 3);
                __builtin_amdgcn_global_load_lds((const GAS void*)src, (LAS void*)(lds + w * 20480 + i * 1024), 16, 0, 0);
            }
            if (WL3 || (NLATE == 0 && jm < 0)) { bar(); bar(); }
            const u32 tb1 = now() + DLY_RB;
            DRAIN();
            const LAS char* tb = lds + w * 20480 + (8 * q4 + (col >> 2)) * 128 + (col & 3) * 8;
#pragma unroll
            for (int ks = 0; ks < 5; ++ks)
#pragma unroll
                for (int tl = 0; tl < 4; ++tl) {
                    const s4 lo = __builtin_amdgcn_ds_read_tr16_b64_v4i16((LAS s4*)(tb + ks * 4096 + tl * 32));
                    const s4 hi = __builtin_amdgcn_ds_read_tr16_b64_v4i16((LAS s4*)(tb + ks * 4096 + tl * 32 + 512));
                    const v2u l2 = __builtin_bit_cast(v2u, lo), h2 = __builtin_bit_cast(v2u, hi);
                    const v4u f = {l2.x, l2.y, h2.x, h2.y};
                    wf[ks * 4 + tl] = __builtin_bit_cast(bfx8, f);
                }
            STAMP(2);
            if (w == 0) {
                wait_until(tlast() + DLY_R); wait_until(tb1);
                STAMP(7);
                top2();
                if (lane == 0) { const v2u g = {g1, g2}; *(LAS v2u*)(ctl + 8) = g; }
            } else {
                u64 g;
                while ((g = __hip_atomic_load((LAS u64*)(ctl + 8), __ATOMIC_RELAXED, __HIP_MEMORY_SCOPE_WORKGROUP)) == 0) {}
                g1 = (u32)g; g2 = (u32)(g >> 32);
            }
            STAMP(9);
            WINNERS();
            v4u hb[5];
            const u32 off = (col == 0 ? row1 : row2) + (160 * w + 8 * q4) * 2;
#pragma unroll
            for (int i = 0; i < 5; ++i) hb[i] = col < 2 ? (v4u){POISON, POISON, POISON, POISON} : z4;
            for (;;) {
                CFENCE();
                bool ok = true;
#pragma unroll
                for (int i = 0; i < 5; ++i)
                    if (!clean4(hb[i])) hb[i] = __builtin_amdgcn_raw_buffer_load_b128(wr, off + i * 64, 0, SC1);
#pragma unroll
                for (int i = 0; i < 5; ++i) ok &= clean4(hb[i]);
                if (__all(ok) || past(tmo)) break;
            }
            STAMP(10);
            f4 acc[4];
#pragma unroll
            for (int tl = 0; tl < 4; ++tl) {
                acc[tl] = (f4){0.f, 0.f, 0.f, 0.f};
#pragma unroll
                for (int ks = 0; ks < 5; ++ks) acc[tl] = __builtin_amdgcn_mfma_f32_16x16x32_bf16(wf[ks * 4 + tl], __builtin_bit_cast(bfx8, hb[ks]), acc[tl], 0, 0, 0);
                if (col < 2) *(LAS f4*)(lds + L_RED + w * 512 + col * 256 + tl * 64 + q4 * 16) = acc[tl];
            }
            if (last()) {
                const LAS f2* q = (const LAS f2*)(lds + L_RED + ((lane >> 4) & 1) * 256 + (lane & 15) * 8);
                const f2 g = rnd((q[0] + q[64]) + (q[128] + q[192])), u = rnd((q[16] + q[80]) + (q[144] + q[208]));
                const f2 sl = {g.x * __builtin_amdgcn_rcpf(1.f + __expf(-g.x)), g.y * __builtin_amdgcn_rcpf(1.f + __expf(-g.y))};
                __builtin_amdgcn_raw_buffer_store_b32(san(pk2(rnd(sl) * u)), wr, W_A + (lane >> 5) * AS + f3 * 128 + (lane & 31) * 4, 0, SC1);
            }
            STAMP(11);
        } else if (isP4) {
            bfx8 wf[14];
            const int ks0 = 14 * w - (w > 2 ? 1 : 0);
            if (WL4) wl(); else { bar(); bar(); }
#pragma unroll
            for (int i = 0; i < 14; ++i) {
                const int ks = ks0 + i < 54 ? ks0 + i : 53;
                const u16* src = p.wd + (32 * ks + (lane >> 1)) * D_ + 16 * g4 + 8 * (lane & 1);
                __builtin_amdgcn_global_load_lds((const GAS void*)src, (LAS void*)(lds + w * 14336 + i * 1024), 16, 0, 0);
            }
            if (WL4) { bar(); bar(); }
            DRAIN();
            const LAS char* tb = lds + w * 14336 + (8 * q4 + (col >> 2)) * 32 + (col & 3) * 8;
#pragma unroll
            for (int i = 0; i < 14; ++i) {
                const s4 lo = __builtin_amdgcn_ds_read_tr16_b64_v4i16((LAS s4*)(tb + i * 1024));
                const s4 hi = __builtin_amdgcn_ds_read_tr16_b64_v4i16((LAS s4*)(tb + i * 1024 + 128));
                const v2u l2 = __builtin_bit_cast(v2u, lo), h2 = __builtin_bit_cast(v2u, hi);
                const v4u f = {l2.x, l2.y, h2.x, h2.y};
                wf[i] = __builtin_bit_cast(bfx8, f);
            }
            STAMP(2);
            const v4u z4 = {0, 0, 0, 0};
            {
                u32 t;
                while ((t = __hip_atomic_load(ctl + 10, __ATOMIC_RELAXED, __HIP_MEMORY_SCOPE_WORKGROUP)) == 0) __builtin_amdgcn_s_sleep(1);
                wait_until(__builtin_amdgcn_readfirstlane(t) + DLY_D);
            }
            STAMP(10);
            const int nks = w < 2 ? 14 : 13;
            v4u av[14], a0_, a1_, b0_, b1_;
            const u32 oa = W_A + (b & 1) * AS + ks0 * 128 + lane * 16, ob = lane < 8 * nks - 64 ? oa + 1024 : oa;
            a0_ = __builtin_amdgcn_raw_buffer_load_b128(wr, oa, 0, SC1); a1_ = __builtin_amdgcn_raw_buffer_load_b128(wr, ob, 0, SC1);
            for (;;) {
                __builtin_amdgcn_s_sleep(STG_A);
                CFENCE();
                b0_ = __builtin_amdgcn_raw_buffer_load_b128(wr, oa, 0, SC1); b1_ = __builtin_amdgcn_raw_buffer_load_b128(wr, ob, 0, SC1);
                CFENCE();
                if (__all(clean4(a0_) && clean4(a1_)) || past(tmo)) break;
                a0_ = __builtin_amdgcn_raw_buffer_load_b128(wr, oa, 0, SC1); a1_ = __builtin_amdgcn_raw_buffer_load_b128(wr, ob, 0, SC1);
                CFENCE();
                if (__all(clean4(b0_) && clean4(b1_))) { a0_ = b0_; a1_ = b1_; break; }
            }
            {
                LAS char* ab_ = lds + w * 14336;
                if (lane >= 8 * nks - 64) a1_ = z4;
                *(LAS v4u*)(ab_ + lane * 16) = a0_;
                *(LAS v4u*)(ab_ + 1024 + lane * 16) = a1_;
                CFENCE();
#pragma unroll
                for (int i = 0; i < 14; ++i) av[i] = *(const LAS v4u*)(ab_ + i * 128 + (col & 1) * 64 + q4 * 16);
            }
            STAMP(11);
            f4 a0 = {0.f, 0.f, 0.f, 0.f}, a1 = {0.f, 0.f, 0.f, 0.f};
#pragma unroll
            for (int i = 0; i < 14; i += 2) {
                a0 = __builtin_amdgcn_mfma_f32_16x16x32_bf16(wf[i], __builtin_bit_cast(bfx8, av[i]), a0, 0, 0, 0);
                a1 = __builtin_amdgcn_mfma_f32_16x16x32_bf16(wf[i + 1], __builtin_bit_cast(bfx8, av[i + 1]), a1, 0, 0, 0);
            }
            if (col < 2) *(LAS f4*)(lds + L_RED + w * 128 + col * 64 + q4 * 16) = a0 + a1;
            if (last() && lane < 16) {
                while (__hip_atomic_load(ctl + 9, WG) == 0) {}
                CFENCE();
                const LAS f2* q = (const LAS f2*)(lds + L_RED + (lane >> 3) * 64 + (lane & 7) * 8);
                const f2 y = (q[0] + q[16]) + (q[32] + q[48]);
                const v4u e = *(const LAS v4u*)(lds + L_RED + 1024 + lane * 16);
                const u32 gw = *(const LAS u32*)(lds + L_MOD + 5 * 1280 + (16 * g4 + 2 * (lane & 7)) * 2);
                const f2 o = rnd(bfup(gw) * rnd(bfup(e.y) + rnd(rnd(y) * __uint_as_float(e.z)))) + bfup(e.x);
                __hip_atomic_store((GAS u32*)(p.out + e.w), pk2(o), RLX);
            }
            STAMP(12);
        }
        else { bar(); bar(); }
    }
}

extern "C" void kernel_launch(void* const* d_in, const int* in_sizes, int n_in,
                              void* d_out, int out_size, void* d_ws, size_t ws_size,
                              hipStream_t stream) {
    Params p;
    p.x = (const u16*)d_in[0]; p.k = (const u16*)d_in[1]; p.v = (const u16*)d_in[2];
    p.sh = (const u16*)d_in[3]; p.sk = (const u16*)d_in[4]; p.sD = (const u16*)d_in[5];
    p.wr = (const u16*)d_in[7]; p.wg = (const u16*)d_in[8]; p.wu = (const u16*)d_in[9]; p.wd = (const u16*)d_in[10];
    p.c = (const u16*)d_in[11]; p.wm = (const u16*)d_in[12];
    p.out = (u16*)d_out; p.ws = (char*)d_ws;
    fused_kernel<<<256, 576, 0, stream>>>(p);
}
```
